```python
import torch

def _softmax(x, axis=-1):
    return torch.softmax(x, dim=axis)
def _gelu(x):
    return torch.nn.functional.gelu(x, approximate="tanh")
def _silu(x):
    return torch.nn.functional.silu(x)
def _sigmoid(x):
    return torch.sigmoid(x)
def _layernorm(x, axis=-1, eps=1e-5):
    m = x.mean(dim=axis, keepdim=True)
    v = x.var(dim=axis, keepdim=True, unbiased=False)
    return (x - m) / torch.sqrt(v + eps)
def _scatter_add(tgt, idx, src, axis=0):
    return tgt.clone().index_add_(axis, idx, src)
def _segment_sum(src, seg, n):
    out = torch.zeros((n,) + src.shape[1:], dtype=src.dtype)
    return out.index_add_(0, seg, src)
def _pad(x, pw):
    flat = tuple(v for pair in reversed(pw) for v in pair)
    return torch.nn.functional.pad(x, flat)

kernel_name = 'concat_heads__sliding_block_cached__sliding_block_cached__decode'

def setup_inputs():

    _w = (lambda a: (lambda t: t.to(torch.bfloat16) if t.is_floating_point() else t)(torch.as_tensor(a)))
    def _A_setup():
        B, Sq, D = 2, 1, 512
        Hq, Hkv, Sk, Dh = 32, 32, 8192, 128
        assert Hq % Hkv == 0, (Hq, Hkv)
        return {
            "x": _w((lambda *s: torch.randn(*s, dtype=torch.bfloat16) * 0.8)(B, Sq, D)),
            "k_cache": _w((lambda *s: torch.randn(*s, dtype=torch.bfloat16) * 1.6)(B, Hkv, Sk, Dh)),
            "v_cache": _w((lambda *s: torch.randn(*s, dtype=torch.bfloat16) * 0.2)(B, Hkv, Sk, Dh)),
            "wq": _w((lambda *s: torch.randn(*s, dtype=torch.bfloat16) * 1.6)(D, Hq * Dh) * (D ** -0.5)),
            "wo": _w((lambda *s: torch.randn(*s, dtype=torch.bfloat16) * 0.2)(Hq * Dh, D) * ((Hq * Dh) ** -0.5)),
        }
    _A_in = _A_setup()

    def _B_setup():
        B, Sq, D = 2, 1, 512
        Hq, Hkv, Sk, Dh = 32, 32, 8192, 128
        assert Hq % Hkv == 0, (Hq, Hkv)
        return {
            "x": _w((lambda *s: torch.randn(*s, dtype=torch.bfloat16) * 0.8)(B, Sq, D)),
            "k_cache": _w((lambda *s: torch.randn(*s, dtype=torch.bfloat16) * 1.6)(B, Hkv, Sk, Dh)),
            "v_cache": _w((lambda *s: torch.randn(*s, dtype=torch.bfloat16) * 0.2)(B, Hkv, Sk, Dh)),
            "wq": _w((lambda *s: torch.randn(*s, dtype=torch.bfloat16) * 1.6)(D, Hq * Dh) * (D ** -0.5)),
            "wo": _w((lambda *s: torch.randn(*s, dtype=torch.bfloat16) * 0.2)(Hq * Dh, D) * ((Hq * Dh) ** -0.5)),
        }
    _B_in = _B_setup()

    return {
        "A_x": _A_in["x"],
        "A_k_cache": _A_in["k_cache"],
        "A_v_cache": _A_in["v_cache"],
        "A_wq": _A_in["wq"],
        "A_wo": _A_in["wo"],
        "B_x": _B_in["x"],
        "B_k_cache": _B_in["k_cache"],
        "B_v_cache": _B_in["v_cache"],
        "B_wq": _B_in["wq"],
        "B_wo": _B_in["wo"],
    }

def reference(A_x, A_k_cache, A_v_cache, A_wq, A_wo, B_x, B_k_cache, B_v_cache, B_wq, B_wo):
    def _A_ref(x, k_cache, v_cache, wq, wo):
        B, Sq, D = x.shape
        Hq, Dh = 32, 128
        Hkv, Sk = k_cache.shape[1], k_cache.shape[2]
        W = min(4096, max(256, Sk // 4))
        rep = Hq // Hkv
        q = (lambda x, *d: x.permute(*d))((x @ wq).reshape(B, Sq, Hq, Dh), 0, 2, 1, 3)
        k = (lambda x, n, axis: torch.repeat_interleave(x, n, dim=axis))(k_cache[:, :, Sk - W:, :], rep, axis=1)
        v = (lambda x, n, axis: torch.repeat_interleave(x, n, dim=axis))(v_cache[:, :, Sk - W:, :], rep, axis=1)
        scores = torch.einsum("bhqd,bhkd->bhqk", q, k) * (Dh ** -0.5)
        p = _softmax(scores, axis=-1)
        o = torch.einsum("bhqk,bhkd->bhqd", p, v)
        return (lambda x, *d: x.permute(*d))(o, 0, 2, 1, 3).reshape(B, Sq, Hq * Dh) @ wo

    def _B_ref(x, k_cache, v_cache, wq, wo):
        B, Sq, D = x.shape
        Hq, Dh = 32, 128
        Hkv, Sk = k_cache.shape[1], k_cache.shape[2]
        W = min(4096, max(256, Sk // 4))
        rep = Hq // Hkv
        q = (lambda x, *d: x.permute(*d))((x @ wq).reshape(B, Sq, Hq, Dh), 0, 2, 1, 3)
        k = (lambda x, n, axis: torch.repeat_interleave(x, n, dim=axis))(k_cache[:, :, Sk - W:, :], rep, axis=1)
        v = (lambda x, n, axis: torch.repeat_interleave(x, n, dim=axis))(v_cache[:, :, Sk - W:, :], rep, axis=1)
        scores = torch.einsum("bhqd,bhkd->bhqk", q, k) * (Dh ** -0.5)
        p = _softmax(scores, axis=-1)
        o = torch.einsum("bhqk,bhkd->bhqd", p, v)
        return (lambda x, *d: x.permute(*d))(o, 0, 2, 1, 3).reshape(B, Sq, Hq * Dh) @ wo

    a = _A_ref(A_x, A_k_cache, A_v_cache, A_wq, A_wo)
    b = _B_ref(B_x, B_k_cache, B_v_cache, B_wq, B_wo)
    return torch.concatenate([a, b], axis=-1)
```

```cpp
#include <hip/hip_runtime.h>
#include <hip/hip_bf16.h>
#include <stdint.h>

namespace {

typedef unsigned v4u __attribute__((ext_vector_type(4)));
typedef unsigned v2u __attribute__((ext_vector_type(2)));
typedef float f32x4 __attribute__((ext_vector_type(4)));
typedef float f32x2 __attribute__((ext_vector_type(2)));
typedef short bf16x8 __attribute__((ext_vector_type(8)));
typedef __bf16 bf16x2 __attribute__((ext_vector_type(2)));

constexpr int NB = 2, DM = 512, NH = 32, SK = 8192, DH = 128, WIN = 2048;
constexpr int NWG = 256, NTHR = 512, NWAVE = 8;
constexpr int KEYS_WG = WIN / 2, KEYS_WAVE = KEYS_WG / NWAVE;
constexpr int NKV = KEYS_WAVE / 4;
constexpr int NW = 16;

constexpr int WS_EPOCH = 0;
constexpr int WS_ML = 4096;
constexpr int WS_BOX = 65536;
constexpr int WS_BYTES = WS_BOX + 4 * 64 * 64 * 64;

#define AUX_KV 2
#define AUX_W 0
constexpr int AUX_SC1 = 16;
constexpr int RSRC_W3 = 0x00020000;
constexpr unsigned SPIN_CAP = 1u << 20;

struct Params {
  const void* x[2];
  const void* k[2];
  const void* v[2];
  const void* wq[2];
  const void* wo[2];
  void* out;
  void* ws;
};

__device__ __forceinline__ f32x4 mfma(v4u a, v4u b, f32x4 c) {
  return __builtin_amdgcn_mfma_f32_16x16x32_bf16(__builtin_bit_cast(bf16x8, a), __builtin_bit_cast(bf16x8, b), c, 0, 0, 0);
}
template <int CTRL> __device__ __forceinline__ unsigned dppu(unsigned x) {
  return (unsigned)__builtin_amdgcn_mov_dpp((int)x, CTRL, 0xf, 0xf, true);
}
template <int CTRL> __device__ __forceinline__ float dppf(float x) {
  return __builtin_bit_cast(float, __builtin_amdgcn_mov_dpp(__builtin_bit_cast(int, x), CTRL, 0xf, 0xf, true));
}
constexpr int ROR1 = 0x121, ROR2 = 0x122, ROR4 = 0x124, ROR8 = 0x128, BCAST = 0x150;
__device__ __forceinline__ unsigned pack_bf16(float lo, float hi) {
  bf16x2 r = {(__bf16)lo, (__bf16)hi};
  return __builtin_bit_cast(unsigned, r);
}
__device__ __forceinline__ float xrow_max(float x) {
  auto s = __builtin_amdgcn_permlane16_swap(__float_as_uint(x), __float_as_uint(x), false, false);
  x = fmaxf(__uint_as_float(s[0]), __uint_as_float(s[1]));
  auto t = __builtin_amdgcn_permlane32_swap(__float_as_uint(x), __float_as_uint(x), false, false);
  return fmaxf(__uint_as_float(t[0]), __uint_as_float(t[1]));
}
__device__ __forceinline__ float xrow_sum(float x) {
  auto s = __builtin_amdgcn_permlane16_swap(__float_as_uint(x), __float_as_uint(x), false, false);
  x = __uint_as_float(s[0]) + __uint_as_float(s[1]);
  auto t = __builtin_amdgcn_permlane32_swap(__float_as_uint(x), __float_as_uint(x), false, false);
  return __uint_as_float(t[0]) + __uint_as_float(t[1]);
}
__device__ __forceinline__ v4u spread(unsigned x, const unsigned (&sel)[4]) {
  v4u b;
  b.x = __builtin_amdgcn_perm(0u, x, sel[0]);
  b.y = __builtin_amdgcn_perm(0u, x, sel[1]);
  b.z = __builtin_amdgcn_perm(0u, x, sel[2]);
  b.w = __builtin_amdgcn_perm(0u, x, sel[3]);
  return b;
}
#define SCHED() __builtin_amdgcn_sched_barrier(0)
__device__ __forceinline__ void wg_barrier() {
  asm volatile("s_waitcnt lgkmcnt(0)\n\ts_barrier" ::: "memory");
}

__global__ void __launch_bounds__(NTHR, 2) fused_decode_kernel(Params p) {
  __shared__ __attribute__((aligned(16))) float lds[2 * NWAVE * DH];
  const int tid = threadIdx.x, lane = tid & 63, w = __builtin_amdgcn_readfirstlane(tid >> 6);
  const int g = lane >> 4, n = lane & 15;
  const int bid = blockIdx.x, jj = bid >> 3, grp = (bid & 7) + 8 * (jj >> 2);
  const int br = grp >> 5, head = grp & 31, b = (jj >> 1) & 1, half = jj & 1;
  const int G = br * 2 + b, me = head * 2 + half;

  const size_t kv_off = ((size_t)(b * NH + head) * SK + (SK - WIN) + half * KEYS_WG + w * KEYS_WAVE) * (DH * 2);
  const __amdgpu_buffer_rsrc_t rK = __builtin_amdgcn_make_buffer_rsrc((char*)p.k[br] + kv_off, 0, KEYS_WAVE * DH * 2, RSRC_W3);
  const __amdgpu_buffer_rsrc_t rV = __builtin_amdgcn_make_buffer_rsrc((char*)p.v[br] + kv_off, 0, KEYS_WAVE * DH * 2, RSRC_W3);
  const __amdgpu_buffer_rsrc_t rQ = __builtin_amdgcn_make_buffer_rsrc((char*)p.wq[br], 0, DM * NH * DH * 2, RSRC_W3);
  const __amdgpu_buffer_rsrc_t rO = __builtin_amdgcn_make_buffer_rsrc((char*)p.wo[br] + (size_t)head * DH * DM * 2, 0, DH * DM * 2, RSRC_W3);
  const __amdgpu_buffer_rsrc_t rX = __builtin_amdgcn_make_buffer_rsrc((char*)p.x[br] + b * DM * 2, 0, DM * 2, RSRC_W3);
  const __amdgpu_buffer_rsrc_t rS = __builtin_amdgcn_make_buffer_rsrc((char*)p.ws, 0, WS_BYTES, RSRC_W3);

  v4u xr[2];
  xr[0] = __builtin_amdgcn_raw_buffer_load_b128(rX, (64 * w + 16 * g) * 2, 0, 0);
  xr[1] = __builtin_amdgcn_raw_buffer_load_b128(rX, (64 * w + 16 * g) * 2 + 16, 0, 0);
  v4u wr[NW];
  {
    const int vo = (64 * w + 16 * g) * (NH * DH * 2) + head * (DH * 2) + n * 16;
#pragma unroll
    for (int t = 0; t < NW; ++t) wr[t] = __builtin_amdgcn_raw_buffer_load_b128(rQ, vo, t * (NH * DH * 2), AUX_W);
  }
  const unsigned epoch = __builtin_amdgcn_raw_buffer_load_b32(rS, WS_EPOCH + G * 256, 0, AUX_SC1);
  v4u kv[NKV];
  {
    const int vo = (n >> 2) * 256 + g * 64 + (n & 3) * 16;
#pragma unroll
    for (int t = 0; t < NKV; ++t) kv[t] = __builtin_amdgcn_raw_buffer_load_b128(rK, vo + (t & 3) * 1024, (t >> 2) * 4096, AUX_KV);
  }
  SCHED();

  unsigned sel0[4], sel1[4], selp[4];
#pragma unroll
  for (int i = 0; i < 4; ++i) {
    const bool here = ((n & 7) >> 1) == i;
    const unsigned lo = (n & 1) ? 0x01000c0cu : 0x0c0c0100u, hi = (n & 1) ? 0x03020c0cu : 0x0c0c0302u;
    sel0[i] = (here && n < 8) ? lo : 0x0c0c0c0cu;
    sel1[i] = (here && n < 8) ? hi : 0x0c0c0c0cu;
    selp[i] = here ? (n < 8 ? lo : hi) : 0x0c0c0c0cu;
  }

  {
    f32x4 acc = {0.f, 0.f, 0.f, 0.f};
#pragma unroll
    for (int t = 0; t < NW; ++t) {
      const unsigned xd = xr[t >> 3][(t >> 1) & 3];
      acc = mfma(wr[t], (t & 1) ? spread(xd, sel1) : spread(xd, sel0), acc);
    }
    if (n < 8) {
#pragma unroll
      for (int i = 0; i < 4; ++i) lds[w * DH + 32 * g + 8 * i + n] = acc[i];
    }
  }
  SCHED();
  {
    const int vo = (32 * g + (n >> 3)) * (DM * 2) + w * 128 + (n & 7) * 16;
#pragma unroll
    for (int t = 0; t < NW; ++t) wr[t] = __builtin_amdgcn_raw_buffer_load_b128(rO, vo + (t & 1) * 2048, (t >> 1) * 4096, AUX_W);
  }
  SCHED();
  wg_barrier();
  v4u qb[4];
  {
    f32x4 q0 = {0.f, 0.f, 0.f, 0.f}, q1 = {0.f, 0.f, 0.f, 0.f};
    const float* src = lds + 32 * g + 8 * (n & 3);
#pragma unroll
    for (int ww = 0; ww < NWAVE; ++ww) {
      q0 += *(const f32x4*)(src + ww * DH);
      q1 += *(const f32x4*)(src + ww * DH + 4);
    }
    v4u q;
    q.x = pack_bf16(q0[0], q0[1]);
    q.y = pack_bf16(q0[2], q0[3]);
    q.z = pack_bf16(q1[0], q1[1]);
    q.w = pack_bf16(q1[2], q1[3]);
    const v4u z = {0u, 0u, 0u, 0u};
#pragma unroll
    for (int t = 0; t < 4; ++t) qb[t] = ((n >> 2) == t) ? q : z;
  }
  SCHED();

  float s[NKV / 4];
#pragma unroll
  for (int u = 0; u < NKV / 4; ++u) {
    f32x4 d = {0.f, 0.f, 0.f, 0.f};
#pragma unroll
    for (int t = 0; t < 4; ++t) d = mfma(kv[4 * u + t], qb[t], d);
#pragma unroll
    for (int t = 0; t < 4; ++t) kv[4 * u + t] = __builtin_amdgcn_raw_buffer_load_b128(rV, lane * 16 + t * 1024, u * 4096, AUX_KV);
    s[u] = (dppf<0x00>(d[0]) + dppf<0x55>(d[1])) + (dppf<0xAA>(d[2]) + dppf<0xFF>(d[3]));
    SCHED();
  }
  constexpr float C2 = 0.088388347648318440f * 1.4426950408889634f;
  float m = s[0];
#pragma unroll
  for (int u = 1; u < NKV / 4; ++u) m = fmaxf(m, s[u]);
  m = fmaxf(m, dppf<ROR4>(m));
  m = fmaxf(m, dppf<ROR8>(m));
  m = xrow_max(m) * C2;
  float l = 0.f;
  unsigned pk[NKV / 4];
#pragma unroll
  for (int u = 0; u < NKV / 4; ++u) {
    const float e = __builtin_amdgcn_exp2f(fmaf(s[u], C2, -m));
    l += e;
    const float hi = __uint_as_float(pack_bf16(e, e) & 0xffff0000u);
    pk[u] = pack_bf16(e, e - hi);
  }
  l += dppf<ROR4>(l);
  l += dppf<ROR8>(l);
  l = xrow_sum(l);
  const unsigned tag = epoch + 1u;
  const int ml_off = WS_ML + (G * NH + head) * 256;
  if (lane == 0) {
    const v4u rec = {__float_as_uint(m), tag, __float_as_uint(l), tag};
    __builtin_amdgcn_raw_buffer_store_b128(rec, rS, ml_off + (half * NWAVE + w) * 16, 0, AUX_SC1);
  }
  SCHED();

  f32x4 o = {0.f, 0.f, 0.f, 0.f};
  v4u ml;
#pragma unroll
  for (int u = 0; u < NKV / 4; ++u) {
    o = mfma(kv[4 * u + 0], spread(dppu<BCAST + 0>(pk[u]), selp), o);
    o = mfma(kv[4 * u + 1], spread(dppu<BCAST + 4>(pk[u]), selp), o);
    o = mfma(kv[4 * u + 2], spread(dppu<BCAST + 8>(pk[u]), selp), o);
    o = mfma(kv[4 * u + 3], spread(dppu<BCAST + 12>(pk[u]), selp), o);
    if (u == 3) ml = __builtin_amdgcn_raw_buffer_load_b128(rS, ml_off + n * 16, 0, AUX_SC1);
    SCHED();
  }
  for (unsigned spins = 0; !__all(ml.y == tag && ml.w == tag); ++spins) {
    if (spins > SPIN_CAP) break;
    __builtin_amdgcn_s_sleep(1);
    ml = __builtin_amdgcn_raw_buffer_load_b128(rS, ml_off + n * 16, 0, AUX_SC1);
  }
  {
    const float mi = __uint_as_float(ml.x);
    float M = fmaxf(mi, dppf<ROR1>(mi));
    M = fmaxf(M, dppf<ROR2>(M));
    M = fmaxf(M, dppf<ROR4>(M));
    M = fmaxf(M, dppf<ROR8>(M));
    float L = __uint_as_float(ml.z) * __builtin_amdgcn_exp2f(mi - M);
    L += dppf<0xB1>(L);
    L += dppf<0x4E>(L);
    L += dppf<0x141>(L);
    L += dppf<0x140>(L);
    const float f = __builtin_amdgcn_exp2f(m - M) / L;
    float* dst = lds + NWAVE * DH + w * DH + 32 * g + n;
#pragma unroll
    for (int i = 0; i < 4; ++i) {
      const float t = (o[i] + dppf<ROR8>(o[i])) * f;
      if (n < 8) dst[8 * i] = t;
    }
  }
  wg_barrier();
  unsigned ox;
  {
    f32x2 a = {0.f, 0.f};
#pragma unroll
    for (int ww = 0; ww < NWAVE; ++ww) a += *(const f32x2*)(lds + NWAVE * DH + ww * DH + 2 * lane);
    ox = pack_bf16(a[0], a[1]);
  }
  f32x4 y = {0.f, 0.f, 0.f, 0.f};
#define WO_STEP(t) y = mfma(wr[t], spread(dppu<BCAST + (t)>(ox), selp), y);
  WO_STEP(0) WO_STEP(1) WO_STEP(2) WO_STEP(3) WO_STEP(4) WO_STEP(5) WO_STEP(6) WO_STEP(7)
  WO_STEP(8) WO_STEP(9) WO_STEP(10) WO_STEP(11) WO_STEP(12) WO_STEP(13) WO_STEP(14) WO_STEP(15)
#undef WO_STEP
  {
    const bool own = (g >> 1) == (n >> 3), up = n >= 8;
    float z[4];
#pragma unroll
    for (int i = 0; i < 4; ++i) { const float r = dppf<ROR8>(y[i]); z[i] = own ? y[i] : r; }
    const float A = up ? z[1] : z[0], B = up ? z[3] : z[2];
    auto t = __builtin_amdgcn_permlane32_swap(__float_as_uint(A), __float_as_uint(B), false, false);
    const float val = __uint_as_float(t[0]) + __uint_as_float(t[1]);
    const int cons = 8 * w + 4 * (g & 1) + 2 * (g >> 1) + (n >> 3);
    const v2u gr = {__float_as_uint(val), tag};
    __builtin_amdgcn_raw_buffer_store_b64(gr, rS, WS_BOX + ((G * 64 + cons) * 64 + me) * 64 + (n & 7) * 8, 0, AUX_SC1);
  }
  if (w != 0) return;

  const int box = WS_BOX + (G * 64 + me) * 4096 + lane * 16;
  v4u r[4];
  for (unsigned spins = 0;; ++spins) {
    bool ok = true;
#pragma unroll
    for (int j = 0; j < 4; ++j) r[j] = __builtin_amdgcn_raw_buffer_load_b128(rS, box + j * 1024, 0, AUX_SC1);
#pragma unroll
    for (int j = 0; j < 4; ++j) ok = ok && r[j].y == tag && r[j].w == tag;
    if (__all(ok) || spins > SPIN_CAP) break;
    __builtin_amdgcn_s_sleep(1);
  }
  float a0 = (__uint_as_float(r[0].x) + __uint_as_float(r[1].x)) + (__uint_as_float(r[2].x) + __uint_as_float(r[3].x));
  float a1 = (__uint_as_float(r[0].z) + __uint_as_float(r[1].z)) + (__uint_as_float(r[2].z) + __uint_as_float(r[3].z));
  a0 += dppf<ROR4>(a0); a1 += dppf<ROR4>(a1);
  a0 += dppf<ROR8>(a0); a1 += dppf<ROR8>(a1);
  a0 = xrow_sum(a0); a1 = xrow_sum(a1);
  if (lane < 4) ((unsigned*)p.out)[(b * (2 * DM) + br * DM + me * 8) / 2 + lane] = pack_bf16(a0, a1);
  if (me == 0 && lane == 0) __builtin_amdgcn_raw_buffer_store_b32(tag, rS, WS_EPOCH + G * 256, 0, AUX_SC1);
}

}

extern "C" void kernel_launch(void* const* d_in, const int* in_sizes, int n_in, void* d_out, int out_size, void* d_ws,
                              size_t ws_size, hipStream_t stream) {
  Params p;
  for (int br = 0; br < 2; ++br) {
    p.x[br] = d_in[5 * br + 0];
    p.k[br] = d_in[5 * br + 1];
    p.v[br] = d_in[5 * br + 2];
    p.wq[br] = d_in[5 * br + 3];
    p.wo[br] = d_in[5 * br + 4];
  }
  p.out = d_out;
  p.ws = d_ws;
  hipLaunchKernelGGL(fused_decode_kernel, dim3(NWG), dim3(NTHR), 0, stream, p);
}
```

```cpp
#include <hip/hip_runtime.h>
#include <hip/hip_bf16.h>
#include <stdint.h>

namespace {

typedef unsigned v4u __attribute__((ext_vector_type(4)));
typedef unsigned v2u __attribute__((ext_vector_type(2)));
typedef float f32x4 __attribute__((ext_vector_type(4)));
typedef float f32x2 __attribute__((ext_vector_type(2)));
typedef short bf16x8 __attribute__((ext_vector_type(8)));
typedef __bf16 bf16x2 __attribute__((ext_vector_type(2)));

constexpr int NB = 2, DM = 512, NH = 32, SK = 8192, DH = 128, WIN = 2048;
constexpr int NWG = 256, NTHR = 512, NWAVE = 8;
constexpr int KEYS_WG = WIN / 2, KEYS_WAVE = KEYS_WG / NWAVE;
constexpr int NKV = KEYS_WAVE / 4;
constexpr int NW = 16;

constexpr int WS_EPOCH = 0;
constexpr int WS_ML = 4096;
constexpr int WS_BOX = 65536;
constexpr int WS_BYTES = WS_BOX + 4 * 64 * 64 * 64;

#define AUX_KV 2
#define AUX_W 2
#define BAR_A 1
#define WO_EARLY 8
#define WO_SPREAD 8
#define ML_AT 7
constexpr int AUX_SC1 = 16;
constexpr int RSRC_W3 = 0x00020000;
constexpr unsigned SPIN_CAP = 1u << 20;

struct Params {
  const void* x[2];
  const void* k[2];
  const void* v[2];
  const void* wq[2];
  const void* wo[2];
  void* out;
  void* ws;
};

__device__ __forceinline__ f32x4 mfma(v4u a, v4u b, f32x4 c) {
  return __builtin_amdgcn_mfma_f32_16x16x32_bf16(__builtin_bit_cast(bf16x8, a), __builtin_bit_cast(bf16x8, b), c, 0, 0, 0);
}
template <int CTRL> __device__ __forceinline__ unsigned dppu(unsigned x) {
  return (unsigned)__builtin_amdgcn_mov_dpp((int)x, CTRL, 0xf, 0xf, true);
}
template <int CTRL> __device__ __forceinline__ float dppf(float x) {
  return __builtin_bit_cast(float, __builtin_amdgcn_mov_dpp(__builtin_bit_cast(int, x), CTRL, 0xf, 0xf, true));
}
constexpr int ROR1 = 0x121, ROR2 = 0x122, ROR4 = 0x124, ROR8 = 0x128, BCAST = 0x150;
__device__ __forceinline__ unsigned pack_bf16(float lo, float hi) {
  bf16x2 r = {(__bf16)lo, (__bf16)hi};
  return __builtin_bit_cast(unsigned, r);
}
__device__ __forceinline__ float round_bf16(float x) { return __uint_as_float(pack_bf16(0.f, x) & 0xffff0000u); }
__device__ __forceinline__ float xrow_max(float x) {
  auto s = __builtin_amdgcn_permlane16_swap(__float_as_uint(x), __float_as_uint(x), false, false);
  x = fmaxf(__uint_as_float(s[0]), __uint_as_float(s[1]));
  auto t = __builtin_amdgcn_permlane32_swap(__float_as_uint(x), __float_as_uint(x), false, false);
  return fmaxf(__uint_as_float(t[0]), __uint_as_float(t[1]));
}
__device__ __forceinline__ float xrow_sum(float x) {
  auto s = __builtin_amdgcn_permlane16_swap(__float_as_uint(x), __float_as_uint(x), false, false);
  x = __uint_as_float(s[0]) + __uint_as_float(s[1]);
  auto t = __builtin_amdgcn_permlane32_swap(__float_as_uint(x), __float_as_uint(x), false, false);
  return __uint_as_float(t[0]) + __uint_as_float(t[1]);
}
__device__ __forceinline__ v4u spread(unsigned x, const unsigned (&sel)[4]) {
  v4u b;
  b.x = __builtin_amdgcn_perm(0u, x, sel[0]);
  b.y = __builtin_amdgcn_perm(0u, x, sel[1]);
  b.z = __builtin_amdgcn_perm(0u, x, sel[2]);
  b.w = __builtin_amdgcn_perm(0u, x, sel[3]);
  return b;
}
#define SCHED() __builtin_amdgcn_sched_barrier(0)
#define STAMP(i)
#define STAMP_OUT()
__device__ __forceinline__ void wg_barrier() {
  asm volatile("s_waitcnt lgkmcnt(0)\n\ts_barrier" ::: "memory");
}

__global__ void __launch_bounds__(NTHR, 2) fused_decode_kernel(Params p) {
  __shared__ __attribute__((aligned(16))) float lds[2 * NWAVE * DH + NWAVE];
  STAMP(0);
  const int tid = threadIdx.x, lane = tid & 63, w = __builtin_amdgcn_readfirstlane(tid >> 6);
  const int g = lane >> 4, n = lane & 15;
  const int bid = blockIdx.x, jj = bid >> 3, grp = (bid & 7) + 8 * (jj >> 2);
  const int br = grp >> 5, head = grp & 31, b = (jj >> 1) & 1, half = jj & 1;
  const int G = br * 2 + b, me = head * 2 + half;

  const size_t kv_off = ((size_t)(b * NH + head) * SK + (SK - WIN) + half * KEYS_WG) * (DH * 2);
  const __amdgpu_buffer_rsrc_t rK = __builtin_amdgcn_make_buffer_rsrc((char*)p.k[br] + kv_off, 0, KEYS_WG * DH * 2, RSRC_W3);
  const __amdgpu_buffer_rsrc_t rV = __builtin_amdgcn_make_buffer_rsrc((char*)p.v[br] + kv_off, 0, KEYS_WG * DH * 2, RSRC_W3);
  const __amdgpu_buffer_rsrc_t rQ = __builtin_amdgcn_make_buffer_rsrc((char*)p.wq[br], 0, DM * NH * DH * 2, RSRC_W3);
  const __amdgpu_buffer_rsrc_t rO = __builtin_amdgcn_make_buffer_rsrc((char*)p.wo[br] + (size_t)head * DH * DM * 2, 0, DH * DM * 2, RSRC_W3);
  const __amdgpu_buffer_rsrc_t rX = __builtin_amdgcn_make_buffer_rsrc((char*)p.x[br] + b * DM * 2, 0, DM * 2, RSRC_W3);
  const __amdgpu_buffer_rsrc_t rS = __builtin_amdgcn_make_buffer_rsrc((char*)p.ws, 0, WS_BYTES, RSRC_W3);

  v4u xr[2];
  xr[0] = __builtin_amdgcn_raw_buffer_load_b128(rX, (64 * w + 16 * g) * 2, 0, 0);
  xr[1] = __builtin_amdgcn_raw_buffer_load_b128(rX, (64 * w + 16 * g) * 2 + 16, 0, 0);
  const unsigned epoch = __builtin_amdgcn_raw_buffer_load_b32(rS, WS_EPOCH + G * 256, 0, AUX_SC1);
  v4u wr[NW];
  v4u kv[NKV];
  const int kvo = w * 4096 + (n >> 2) * 256 + g * 64 + (n & 3) * 16;
#define LD_K(t) kv[t] = __builtin_amdgcn_raw_buffer_load_b128(rK, kvo + ((t) & 3) * 1024, ((t) >> 2) * 32768, AUX_KV)
  {
    const int vo = (64 * w + 16 * g) * (NH * DH * 2) + head * (DH * 2) + n * 16;
#pragma unroll
    for (int t = 0; t < NW; ++t) {
      wr[t] = __builtin_amdgcn_raw_buffer_load_b128(rQ, vo, t * (NH * DH * 2), AUX_W);
      LD_K(t);
      SCHED();
    }
  }
  asm volatile("s_barrier" ::: "memory");
  SCHED();
  STAMP(1);

  unsigned sel0[4], sel1[4], selp[4];
#pragma unroll
  for (int i = 0; i < 4; ++i) {
    const bool here = ((n & 7) >> 1) == i;
    const unsigned lo = (n & 1) ? 0x01000c0cu : 0x0c0c0100u, hi = (n & 1) ? 0x03020c0cu : 0x0c0c0302u;
    sel0[i] = (here && n < 8) ? lo : 0x0c0c0c0cu;
    sel1[i] = (here && n < 8) ? hi : 0x0c0c0c0cu;
    selp[i] = here ? (n < 8 ? lo : hi) : 0x0c0c0c0cu;
  }

  {
    f32x4 acc = {0.f, 0.f, 0.f, 0.f};
#pragma unroll
    for (int t = 0; t < NW; ++t) {
      const unsigned xd = xr[t >> 3][(t >> 1) & 3];
      acc = mfma(wr[t], (t & 1) ? spread(xd, sel1) : spread(xd, sel0), acc);
    }
    if (n < 8) {
#pragma unroll
      for (int i = 0; i < 4; ++i) lds[w * DH + 32 * g + 8 * i + n] = acc[i];
    }
  }
  SCHED();
  STAMP(2);
  const int woo = (32 * g + (n >> 3)) * (DM * 2) + w * 128 + (n & 7) * 16;
#define LD_O(t) wr[t] = __builtin_amdgcn_raw_buffer_load_b128(rO, woo + ((t) & 1) * 2048, ((t) >> 1) * 4096, AUX_W)
#pragma unroll
  for (int t = 0; t < NW; ++t) {
    if (t < WO_EARLY) LD_O(t);
    LD_K(NW + t);
    SCHED();
  }
  wg_barrier();
  v4u qb[4];
  {
    f32x4 q0 = {0.f, 0.f, 0.f, 0.f}, q1 = {0.f, 0.f, 0.f, 0.f};
    const float* src = lds + 32 * g + 8 * (n & 3);
#pragma unroll
    for (int ww = 0; ww < NWAVE; ++ww) {
      q0 += *(const f32x4*)(src + ww * DH);
      q1 += *(const f32x4*)(src + ww * DH + 4);
    }
    v4u q;
    q.x = pack_bf16(q0[0], q0[1]);
    q.y = pack_bf16(q0[2], q0[3]);
    q.z = pack_bf16(q1[0], q1[1]);
    q.w = pack_bf16(q1[2], q1[3]);
    const v4u z = {0u, 0u, 0u, 0u};
#pragma unroll
    for (int t = 0; t < 4; ++t) qb[t] = ((n >> 2) == t) ? q : z;
  }
  SCHED();

  STAMP(3);
  float s[NKV / 4];
#pragma unroll
  for (int u = 0; u < NKV / 4; ++u) {
    f32x4 d = {0.f, 0.f, 0.f, 0.f};
#pragma unroll
    for (int t = 0; t < 4; ++t) d = mfma(kv[4 * u + t], qb[t], d);
#pragma unroll
    for (int t = 0; t < 4; ++t) kv[4 * u + t] = __builtin_amdgcn_raw_buffer_load_b128(rV, w * 4096 + lane * 16 + t * 1024, u * 32768, AUX_KV);
    const float raw = (dppf<0x00>(d[0]) + dppf<0x55>(d[1])) + (dppf<0xAA>(d[2]) + dppf<0xFF>(d[3]));
    s[u] = round_bf16(round_bf16(raw) * 0.088388347648318440f);
    SCHED();
  }
  constexpr float C2 = 1.4426950408889634f;
  float m = s[0];
#pragma unroll
  for (int u = 1; u < NKV / 4; ++u) m = fmaxf(m, s[u]);
  m = fmaxf(m, dppf<ROR4>(m));
  m = fmaxf(m, dppf<ROR8>(m));
  m = xrow_max(m) * C2;
  float l = 0.f;
  unsigned pk[NKV / 4];
#pragma unroll
  for (int u = 0; u < NKV / 4; ++u) {
    const float e = __builtin_amdgcn_exp2f(fmaf(s[u], C2, -m));
    l += e;
    const float hi = __uint_as_float(pack_bf16(e, e) & 0xffff0000u);
    pk[u] = pack_bf16(e, e - hi);
  }
  l += dppf<ROR4>(l);
  l += dppf<ROR8>(l);
  l = xrow_sum(l);
  const unsigned tag = epoch + 1u;
  const int ml_off = WS_ML + (G * NH + head) * 256;
  if (lane == 0) {
    const v4u rec = {__float_as_uint(m), tag, __float_as_uint(l), tag};
    __builtin_amdgcn_raw_buffer_store_b128(rec, rS, ml_off + (half * NWAVE + w) * 16, 0, AUX_SC1);
  }
  SCHED();

  if (lane == 0) lds[2 * NWAVE * DH + w] = m;
  wg_barrier();
  STAMP(4);
  float mwg;
  {
    const f32x4 m0 = *(const f32x4*)(lds + 2 * NWAVE * DH), m1 = *(const f32x4*)(lds + 2 * NWAVE * DH + 4);
    mwg = fmaxf(fmaxf(fmaxf(m0[0], m0[1]), fmaxf(m0[2], m0[3])), fmaxf(fmaxf(m1[0], m1[1]), fmaxf(m1[2], m1[3])));
  }
  const float fw = __builtin_amdgcn_exp2f(m - mwg);
  f32x4 o = {0.f, 0.f, 0.f, 0.f};
  v4u ml;
#pragma unroll
  for (int u = 0; u < NKV / 4; ++u) {
#pragma unroll
    for (int t = WO_EARLY + (u * (NW - WO_EARLY)) / WO_SPREAD; t < WO_EARLY + ((u + 1) * (NW - WO_EARLY)) / WO_SPREAD && t < NW; ++t) LD_O(t);
    if (u == ML_AT) ml = __builtin_amdgcn_raw_buffer_load_b128(rS, ml_off + n * 16, 0, AUX_SC1);
    SCHED();
    o = mfma(kv[4 * u + 0], spread(dppu<BCAST + 0>(pk[u]), selp), o);
    o = mfma(kv[4 * u + 1], spread(dppu<BCAST + 4>(pk[u]), selp), o);
    o = mfma(kv[4 * u + 2], spread(dppu<BCAST + 8>(pk[u]), selp), o);
    o = mfma(kv[4 * u + 3], spread(dppu<BCAST + 12>(pk[u]), selp), o);
    SCHED();
  }
  STAMP(5);
  {
    float* dst = lds + NWAVE * DH + w * DH + 32 * g + (n & 7) + (n >> 3) * 16;
    const float t0 = (o[0] + dppf<ROR8>(o[0])) * fw, t1 = (o[1] + dppf<ROR8>(o[1])) * fw;
    const float t2 = (o[2] + dppf<ROR8>(o[2])) * fw, t3 = (o[3] + dppf<ROR8>(o[3])) * fw;
    dst[0] = n < 8 ? t0 : t2;
    dst[8] = n < 8 ? t1 : t3;
  }
  wg_barrier();
  unsigned ox;
  {
    f32x2 a = {0.f, 0.f};
#pragma unroll
    for (int ww = 0; ww < NWAVE; ++ww) a += *(const f32x2*)(lds + NWAVE * DH + ww * DH + 2 * lane);
    ox = pack_bf16(a[0], a[1]);
  }
  f32x4 y = {0.f, 0.f, 0.f, 0.f};
#define WO_STEP(t) y = mfma(wr[t], spread(dppu<BCAST + (t)>(ox), selp), y);
  WO_STEP(0) WO_STEP(1) WO_STEP(2) WO_STEP(3) WO_STEP(4) WO_STEP(5) WO_STEP(6) WO_STEP(7)
  WO_STEP(8) WO_STEP(9) WO_STEP(10) WO_STEP(11) WO_STEP(12) WO_STEP(13) WO_STEP(14) WO_STEP(15)
#undef WO_STEP
  for (unsigned spins = 0; !__all(ml.y == tag && ml.w == tag); ++spins) {
    if (spins > SPIN_CAP) break;
    __builtin_amdgcn_s_sleep(1);
    asm volatile("" ::: "memory");
    ml = __builtin_amdgcn_raw_buffer_load_b128(rS, ml_off + n * 16, 0, AUX_SC1);
  }
  float fin;
  {
    const float mi = __uint_as_float(ml.x);
    float M = fmaxf(mi, dppf<ROR1>(mi));
    M = fmaxf(M, dppf<ROR2>(M));
    M = fmaxf(M, dppf<ROR4>(M));
    M = fmaxf(M, dppf<ROR8>(M));
    float L = __uint_as_float(ml.z) * __builtin_amdgcn_exp2f(mi - M);
    L += dppf<0xB1>(L);
    L += dppf<0x4E>(L);
    L += dppf<0x141>(L);
    L += dppf<0x140>(L);
    fin = __builtin_amdgcn_exp2f(mwg - M) / L;
  }
  {
    const bool own = (g >> 1) == (n >> 3), up = n >= 8;
    float z[4];
#pragma unroll
    for (int i = 0; i < 4; ++i) { const float r = dppf<ROR8>(y[i]); z[i] = own ? y[i] : r; }
    const float A = up ? z[1] : z[0], B = up ? z[3] : z[2];
    auto t = __builtin_amdgcn_permlane32_swap(__float_as_uint(A), __float_as_uint(B), false, false);
    const float val = (__uint_as_float(t[0]) + __uint_as_float(t[1])) * fin;
    const int cons = 8 * w + 4 * (g & 1) + 2 * (g >> 1) + (n >> 3);
    const v2u gr = {__float_as_uint(val), tag};
    __builtin_amdgcn_raw_buffer_store_b64(gr, rS, WS_BOX + ((G * 64 + cons) * 64 + me) * 64 + (n & 7) * 8, 0, AUX_SC1);
  }
  STAMP(6);
  if (w != 0) { STAMP_OUT(); return; }

  const int box = WS_BOX + (G * 64 + me) * 4096 + lane * 16;
  v4u r[4];
  for (unsigned spins = 0;; ++spins) {
    bool ok = true;
    asm volatile("" ::: "memory");
#pragma unroll
    for (int j = 0; j < 4; ++j) r[j] = __builtin_amdgcn_raw_buffer_load_b128(rS, box + j * 1024, 0, AUX_SC1);
#pragma unroll
    for (int j = 0; j < 4; ++j) ok = ok && r[j].y == tag && r[j].w == tag;
    if (__all(ok) || spins > SPIN_CAP) break;
    __builtin_amdgcn_s_sleep(1);
  }
  float a0 = (__uint_as_float(r[0].x) + __uint_as_float(r[1].x)) + (__uint_as_float(r[2].x) + __uint_as_float(r[3].x));
  float a1 = (__uint_as_float(r[0].z) + __uint_as_float(r[1].z)) + (__uint_as_float(r[2].z) + __uint_as_float(r[3].z));
  a0 += dppf<ROR4>(a0); a1 += dppf<ROR4>(a1);
  a0 += dppf<ROR8>(a0); a1 += dppf<ROR8>(a1);
  a0 = xrow_sum(a0); a1 = xrow_sum(a1);
  if (lane < 4) ((unsigned*)p.out)[(b * (2 * DM) + br * DM + me * 8) / 2 + lane] = pack_bf16(a0, a1);
  STAMP(7);
  STAMP_OUT();
  if (me == 0 && lane == 0) __builtin_amdgcn_raw_buffer_store_b32(tag, rS, WS_EPOCH + G * 256, 0, AUX_SC1);
}

}

extern "C" void kernel_launch(void* const* d_in, const int* in_sizes, int n_in, void* d_out, int out_size, void* d_ws,
                              size_t ws_size, hipStream_t stream) {
  Params p;
  for (int br = 0; br < 2; ++br) {
    p.x[br] = d_in[5 * br + 0];
    p.k[br] = d_in[5 * br + 1];
    p.v[br] = d_in[5 * br + 2];
    p.wq[br] = d_in[5 * br + 3];
    p.wo[br] = d_in[5 * br + 4];
  }
  p.out = d_out;
  p.ws = d_ws;
  hipLaunchKernelGGL(fused_decode_kernel, dim3(NWG), dim3(NTHR), 0, stream, p);
}
```

```cpp
#define DIAG_BID 0
#define DIAG_K 0
#define DIAG_I 0
#include <hip/hip_runtime.h>
#include <hip/hip_bf16.h>
#include <stdint.h>
#include <stdio.h>

namespace {

typedef unsigned v4u __attribute__((ext_vector_type(4)));
typedef unsigned v2u __attribute__((ext_vector_type(2)));
typedef float f32x4 __attribute__((ext_vector_type(4)));
typedef float f32x2 __attribute__((ext_vector_type(2)));
typedef short bf16x8 __attribute__((ext_vector_type(8)));
typedef __bf16 bf16x2 __attribute__((ext_vector_type(2)));

constexpr int NB = 2, DM = 512, NH = 32, SK = 8192, DH = 128, WIN = 2048;
constexpr int NWG = 256, NTHR = 512, NWAVE = 8;
constexpr int KEYS_WG = WIN / 2, KEYS_WAVE = KEYS_WG / NWAVE;
constexpr int NKV = KEYS_WAVE / 4;
constexpr int NW = 16;

constexpr int WS_EPOCH = 0;
constexpr int WS_ML = 4096;
constexpr int WS_BOX = 65536;
constexpr int WS_BYTES = WS_BOX + 4 * 64 * 64 * 64;

#define AUX_KV 2
#define AUX_W 2
#define BAR_A 1
#define WO_EARLY 8
#define WO_SPREAD 8
#define ML_AT 7
constexpr int AUX_SC1 = 16;
constexpr int RSRC_W3 = 0x00020000;
constexpr unsigned SPIN_CAP = 1u << 20;

struct Params {
  const void* x[2];
  const void* k[2];
  const void* v[2];
  const void* wq[2];
  const void* wo[2];
  void* out;
  void* ws;
};

__device__ __forceinline__ f32x4 mfma(v4u a, v4u b, f32x4 c) {
  return __builtin_amdgcn_mfma_f32_16x16x32_bf16(__builtin_bit_cast(bf16x8, a), __builtin_bit_cast(bf16x8, b), c, 0, 0, 0);
}
template <int CTRL> __device__ __forceinline__ unsigned dppu(unsigned x) {
  return (unsigned)__builtin_amdgcn_mov_dpp((int)x, CTRL, 0xf, 0xf, true);
}
template <int CTRL> __device__ __forceinline__ float dppf(float x) {
  return __builtin_bit_cast(float, __builtin_amdgcn_mov_dpp(__builtin_bit_cast(int, x), CTRL, 0xf, 0xf, true));
}
constexpr int ROR1 = 0x121, ROR2 = 0x122, ROR4 = 0x124, ROR8 = 0x128, BCAST = 0x150;
__device__ __forceinline__ unsigned pack_bf16(float lo, float hi) {
  bf16x2 r = {(__bf16)lo, (__bf16)hi};
  return __builtin_bit_cast(unsigned, r);
}
__device__ __forceinline__ float round_bf16(float x) { return __uint_as_float(pack_bf16(0.f, x) & 0xffff0000u); }
__device__ __forceinline__ float xrow_max(float x) {
  auto s = __builtin_amdgcn_permlane16_swap(__float_as_uint(x), __float_as_uint(x), false, false);
  x = fmaxf(__uint_as_float(s[0]), __uint_as_float(s[1]));
  auto t = __builtin_amdgcn_permlane32_swap(__float_as_uint(x), __float_as_uint(x), false, false);
  return fmaxf(__uint_as_float(t[0]), __uint_as_float(t[1]));
}
__device__ __forceinline__ float xrow_sum(float x) {
  auto s = __builtin_amdgcn_permlane16_swap(__float_as_uint(x), __float_as_uint(x), false, false);
  x = __uint_as_float(s[0]) + __uint_as_float(s[1]);
  auto t = __builtin_amdgcn_permlane32_swap(__float_as_uint(x), __float_as_uint(x), false, false);
  return __uint_as_float(t[0]) + __uint_as_float(t[1]);
}
__device__ __forceinline__ v4u spread(unsigned x, const unsigned (&sel)[4]) {
  v4u b;
  b.x = __builtin_amdgcn_perm(0u, x, sel[0]);
  b.y = __builtin_amdgcn_perm(0u, x, sel[1]);
  b.z = __builtin_amdgcn_perm(0u, x, sel[2]);
  b.w = __builtin_amdgcn_perm(0u, x, sel[3]);
  return b;
}
#define SCHED() __builtin_amdgcn_sched_barrier(0)
#define PROF_STAMPS
#define PROF_NS 8
#define PROF_OFF (4 << 20)
#define STAMP(i) do { SCHED(); asm volatile("s_memrealtime %0\n\ts_waitcnt lgkmcnt(0)" : "=s"(stamp[i]) :: "memory"); SCHED(); } while (0)
#define STAMP_OUT() do { if (lane == 0) { unsigned long long* d = (unsigned long long*)((char*)p.ws + PROF_OFF) + (size_t)(bid * 8 + w) * PROF_NS; for (int i_ = 0; i_ < PROF_NS; ++i_) d[i_] = stamp[i_]; } } while (0)
__device__ __forceinline__ void wg_barrier() {
  asm volatile("s_waitcnt lgkmcnt(0)\n\ts_barrier" ::: "memory");
}

__global__ void __launch_bounds__(NTHR, 2) fused_decode_kernel(Params p) {
  __shared__ __attribute__((aligned(16))) float lds[2 * NWAVE * DH + NWAVE];
  unsigned long long stamp[PROF_NS] = {0, 0, 0, 0, 0, 0, 0, 0};
  STAMP(0);
  const int tid = threadIdx.x, lane = tid & 63, w = __builtin_amdgcn_readfirstlane(tid >> 6);
  const int g = lane >> 4, n = lane & 15;
  const int bid = blockIdx.x, jj = bid >> 3, grp = (bid & 7) + 8 * (jj >> 2);
  const int br = grp >> 5, head = grp & 31, b = (jj >> 1) & 1, half = jj & 1;
  const int G = br * 2 + b, me = head * 2 + half;

  const size_t kv_off = ((size_t)(b * NH + head) * SK + (SK - WIN) + half * KEYS_WG) * (DH * 2);
  const __amdgpu_buffer_rsrc_t rK = __builtin_amdgcn_make_buffer_rsrc((char*)p.k[br] + kv_off, 0, KEYS_WG * DH * 2, RSRC_W3);
  const __amdgpu_buffer_rsrc_t rV = __builtin_amdgcn_make_buffer_rsrc((char*)p.v[br] + kv_off, 0, KEYS_WG * DH * 2, RSRC_W3);
  const __amdgpu_buffer_rsrc_t rQ = __builtin_amdgcn_make_buffer_rsrc((char*)p.wq[br], 0, DM * NH * DH * 2, RSRC_W3);
  const __amdgpu_buffer_rsrc_t rO = __builtin_amdgcn_make_buffer_rsrc((char*)p.wo[br] + (size_t)head * DH * DM * 2, 0, DH * DM * 2, RSRC_W3);
  const __amdgpu_buffer_rsrc_t rX = __builtin_amdgcn_make_buffer_rsrc((char*)p.x[br] + b * DM * 2, 0, DM * 2, RSRC_W3);
  const __amdgpu_buffer_rsrc_t rS = __builtin_amdgcn_make_buffer_rsrc((char*)p.ws, 0, WS_BYTES, RSRC_W3);

  v4u xr[2];
  xr[0] = __builtin_amdgcn_raw_buffer_load_b128(rX, (64 * w + 16 * g) * 2, 0, 0);
  xr[1] = __builtin_amdgcn_raw_buffer_load_b128(rX, (64 * w + 16 * g) * 2 + 16, 0, 0);
  const unsigned epoch = __builtin_amdgcn_raw_buffer_load_b32(rS, WS_EPOCH + G * 256, 0, AUX_SC1);
  v4u wr[NW];
  v4u kv[NKV];
  const int kvo = w * 4096 + (n >> 2) * 256 + g * 64 + (n & 3) * 16;
#define LD_K(t) kv[t] = __builtin_amdgcn_raw_buffer_load_b128(rK, kvo + ((t) & 3) * 1024, ((t) >> 2) * 32768, AUX_KV)
  {
    const int vo = (64 * w + 16 * g) * (NH * DH * 2) + head * (DH * 2) + n * 16;
#pragma unroll
    for (int t = 0; t < NW; ++t) {
      wr[t] = __builtin_amdgcn_raw_buffer_load_b128(rQ, vo, t * (NH * DH * 2), AUX_W);
      LD_K(t);
      SCHED();
    }
  }
  asm volatile("s_barrier" ::: "memory");
  SCHED();
  STAMP(1);

  unsigned sel0[4], sel1[4], selp[4];
#pragma unroll
  for (int i = 0; i < 4; ++i) {
    const bool here = ((n & 7) >> 1) == i;
    const unsigned lo = (n & 1) ? 0x01000c0cu : 0x0c0c0100u, hi = (n & 1) ? 0x03020c0cu : 0x0c0c0302u;
    sel0[i] = (here && n < 8) ? lo : 0x0c0c0c0cu;
    sel1[i] = (here && n < 8) ? hi : 0x0c0c0c0cu;
    selp[i] = here ? (n < 8 ? lo : hi) : 0x0c0c0c0cu;
  }

  {
    f32x4 acc = {0.f, 0.f, 0.f, 0.f};
#pragma unroll
    for (int t = 0; t < NW; ++t) {
      const unsigned xd = xr[t >> 3][(t >> 1) & 3];
      acc = mfma(wr[t], (t & 1) ? spread(xd, sel1) : spread(xd, sel0), acc);
    }
    if (n < 8) {
#pragma unroll
      for (int i = 0; i < 4; ++i) lds[w * DH + 32 * g + 8 * i + n] = acc[i];
    }
  }
  SCHED();
  STAMP(2);
  const int woo = (32 * g + (n >> 3)) * (DM * 2) + w * 128 + (n & 7) * 16;
#define LD_O(t) wr[t] = __builtin_amdgcn_raw_buffer_load_b128(rO, woo + ((t) & 1) * 2048, ((t) >> 1) * 4096, AUX_W)
#pragma unroll
  for (int t = 0; t < NW; ++t) {
    if (t < WO_EARLY) LD_O(t);
    LD_K(NW + t);
    SCHED();
  }
  wg_barrier();
  v4u qb[4];
  {
    f32x4 q0 = {0.f, 0.f, 0.f, 0.f}, q1 = {0.f, 0.f, 0.f, 0.f};
    const float* src = lds + 32 * g + 8 * (n & 3);
#pragma unroll
    for (int ww = 0; ww < NWAVE; ++ww) {
      q0 += *(const f32x4*)(src + ww * DH);
      q1 += *(const f32x4*)(src + ww * DH + 4);
    }
    v4u q;
    q.x = pack_bf16(q0[0], q0[1]);
    q.y = pack_bf16(q0[2], q0[3]);
    q.z = pack_bf16(q1[0], q1[1]);
    q.w = pack_bf16(q1[2], q1[3]);
    const v4u z = {0u, 0u, 0u, 0u};
#pragma unroll
    for (int t = 0; t < 4; ++t) qb[t] = ((n >> 2) == t) ? q : z;
  }
  SCHED();

  STAMP(3);
  float s[NKV / 4];
#pragma unroll
  for (int u = 0; u < NKV / 4; ++u) {
    f32x4 d = {0.f, 0.f, 0.f, 0.f};
#pragma unroll
    for (int t = 0; t < 4; ++t) d = mfma(kv[4 * u + t], qb[t], d);
#pragma unroll
    for (int t = 0; t < 4; ++t) kv[4 * u + t] = __builtin_amdgcn_raw_buffer_load_b128(rV, w * 4096 + lane * 16 + t * 1024, u * 32768, AUX_KV);
    const float raw = (dppf<0x00>(d[0]) + dppf<0x55>(d[1])) + (dppf<0xAA>(d[2]) + dppf<0xFF>(d[3]));
    s[u] = round_bf16(round_bf16(raw) * 0.088388347648318440f);
    SCHED();
  }
  constexpr float C2 = 1.4426950408889634f;
  float m = s[0];
#pragma unroll
  for (int u = 1; u < NKV / 4; ++u) m = fmaxf(m, s[u]);
  m = fmaxf(m, dppf<ROR4>(m));
  m = fmaxf(m, dppf<ROR8>(m));
  m = xrow_max(m) * C2;
  float l = 0.f;
  unsigned pk[NKV / 4];
#pragma unroll
  for (int u = 0; u < NKV / 4; ++u) {
    const float e = __builtin_amdgcn_exp2f(fmaf(s[u], C2, -m));
    l += e;
    const float hi = __uint_as_float(pack_bf16(e, e) & 0xffff0000u);
    pk[u] = pack_bf16(e, e - hi);
  }
  l += dppf<ROR4>(l);
  l += dppf<ROR8>(l);
  l = xrow_sum(l);
  const unsigned tag = epoch + 1u;
  const int ml_off = WS_ML + (G * NH + head) * 256;
  if (lane == 0) {
    const v4u rec = {__float_as_uint(m), tag, __float_as_uint(l), tag};
    __builtin_amdgcn_raw_buffer_store_b128(rec, rS, ml_off + (half * NWAVE + w) * 16, 0, AUX_SC1);
  }
  SCHED();

  if (lane == 0) lds[2 * NWAVE * DH + w] = m;
  wg_barrier();
  STAMP(4);
  float mwg;
  {
    const f32x4 m0 = *(const f32x4*)(lds + 2 * NWAVE * DH), m1 = *(const f32x4*)(lds + 2 * NWAVE * DH + 4);
    mwg = fmaxf(fmaxf(fmaxf(m0[0], m0[1]), fmaxf(m0[2], m0[3])), fmaxf(fmaxf(m1[0], m1[1]), fmaxf(m1[2], m1[3])));
  }
  const float fw = __builtin_amdgcn_exp2f(m - mwg);
  f32x4 o = {0.f, 0.f, 0.f, 0.f};
  v4u ml;
#pragma unroll
  for (int u = 0; u < NKV / 4; ++u) {
#pragma unroll
    for (int t = WO_EARLY + (u * (NW - WO_EARLY)) / WO_SPREAD; t < WO_EARLY + ((u + 1) * (NW - WO_EARLY)) / WO_SPREAD && t < NW; ++t) LD_O(t);
    if (u == ML_AT) ml = __builtin_amdgcn_raw_buffer_load_b128(rS, ml_off + n * 16, 0, AUX_SC1);
    SCHED();
    o = mfma(kv[4 * u + 0], spread(dppu<BCAST + 0>(pk[u]), selp), o);
    o = mfma(kv[4 * u + 1], spread(dppu<BCAST + 4>(pk[u]), selp), o);
    o = mfma(kv[4 * u + 2], spread(dppu<BCAST + 8>(pk[u]), selp), o);
    o = mfma(kv[4 * u + 3], spread(dppu<BCAST + 12>(pk[u]), selp), o);
    SCHED();
  }
  STAMP(5);
  {
    float* dst = lds + NWAVE * DH + w * DH + 32 * g + (n & 7) + (n >> 3) * 16;
    const float t0 = (o[0] + dppf<ROR8>(o[0])) * fw, t1 = (o[1] + dppf<ROR8>(o[1])) * fw;
    const float t2 = (o[2] + dppf<ROR8>(o[2])) * fw, t3 = (o[3] + dppf<ROR8>(o[3])) * fw;
    dst[0] = n < 8 ? t0 : t2;
    dst[8] = n < 8 ? t1 : t3;
  }
  wg_barrier();
  unsigned ox;
  {
    f32x2 a = {0.f, 0.f};
#pragma unroll
    for (int ww = 0; ww < NWAVE; ++ww) a += *(const f32x2*)(lds + NWAVE * DH + ww * DH + 2 * lane);
    ox = pack_bf16(a[0], a[1]);
  }
  f32x4 y = {0.f, 0.f, 0.f, 0.f};
#define WO_STEP(t) y = mfma(wr[t], spread(dppu<BCAST + (t)>(ox), selp), y);
  WO_STEP(0) WO_STEP(1) WO_STEP(2) WO_STEP(3) WO_STEP(4) WO_STEP(5) WO_STEP(6) WO_STEP(7)
  WO_STEP(8) WO_STEP(9) WO_STEP(10) WO_STEP(11) WO_STEP(12) WO_STEP(13) WO_STEP(14) WO_STEP(15)
#undef WO_STEP
  for (unsigned spins = 0; !__all(ml.y == tag && ml.w == tag); ++spins) {
    if (spins > SPIN_CAP) break;
    __builtin_amdgcn_s_sleep(1);
    asm volatile("" ::: "memory");
    ml = __builtin_amdgcn_raw_buffer_load_b128(rS, ml_off + n * 16, 0, AUX_SC1);
  }
  float fin;
  {
    const float mi = __uint_as_float(ml.x);
    float M = fmaxf(mi, dppf<ROR1>(mi));
    M = fmaxf(M, dppf<ROR2>(M));
    M = fmaxf(M, dppf<ROR4>(M));
    M = fmaxf(M, dppf<ROR8>(M));
    float L = __uint_as_float(ml.z) * __builtin_amdgcn_exp2f(mi - M);
    L += dppf<0xB1>(L);
    L += dppf<0x4E>(L);
    L += dppf<0x141>(L);
    L += dppf<0x140>(L);
    fin = __builtin_amdgcn_exp2f(mwg - M) / L;
  }
  {
    const bool own = (g >> 1) == (n >> 3), up = n >= 8;
    float z[4];
#pragma unroll
    for (int i = 0; i < 4; ++i) { const float r = dppf<ROR8>(y[i]); z[i] = own ? y[i] : r; }
    const float A = up ? z[1] : z[0], B = up ? z[3] : z[2];
    auto t = __builtin_amdgcn_permlane32_swap(__float_as_uint(A), __float_as_uint(B), false, false);
    const float val = (__uint_as_float(t[0]) + __uint_as_float(t[1])) * fin;
    const int cons = 8 * w + 4 * (g & 1) + 2 * (g >> 1) + (n >> 3);
    const v2u gr = {__float_as_uint(val), tag};
    __builtin_amdgcn_raw_buffer_store_b64(gr, rS, WS_BOX + ((G * 64 + cons) * 64 + me) * 64 + (n & 7) * 8, 0, AUX_SC1);
  }
  STAMP(6);
  if (w != 0) { return; }

  const int box = WS_BOX + (G * 64 + me) * 4096 + lane * 16;
  v4u r[4];
  for (unsigned spins = 0;; ++spins) {
    bool ok = true;
    asm volatile("" ::: "memory");
#pragma unroll
    for (int j = 0; j < 4; ++j) r[j] = __builtin_amdgcn_raw_buffer_load_b128(rS, box + j * 1024, 0, AUX_SC1);
#pragma unroll
    for (int j = 0; j < 4; ++j) ok = ok && r[j].y == tag && r[j].w == tag;
    if (__all(ok) || spins > SPIN_CAP) break;
    __builtin_amdgcn_s_sleep(1);
  }
  float a0 = (__uint_as_float(r[0].x) + __uint_as_float(r[1].x)) + (__uint_as_float(r[2].x) + __uint_as_float(r[3].x));
  float a1 = (__uint_as_float(r[0].z) + __uint_as_float(r[1].z)) + (__uint_as_float(r[2].z) + __uint_as_float(r[3].z));
  a0 += dppf<ROR4>(a0); a1 += dppf<ROR4>(a1);
  a0 += dppf<ROR8>(a0); a1 += dppf<ROR8>(a1);
  a0 = xrow_sum(a0); a1 = xrow_sum(a1);
  if (lane < 4) ((unsigned*)p.out)[(b * (2 * DM) + br * DM + me * 8) / 2 + lane] = pack_bf16(a0, a1);
  STAMP(7);
  if (bid == DIAG_BID) {
    unsigned long long tgt = stamp[0] + 5000 + DIAG_K * (stamp[DIAG_I] - stamp[0]), now;
    do { asm volatile("s_memrealtime %0\n\ts_waitcnt lgkmcnt(0)" : "=s"(now) :: "memory"); } while (now < tgt);
  }
  if (me == 0 && lane == 0) __builtin_amdgcn_raw_buffer_store_b32(tag, rS, WS_EPOCH + G * 256, 0, AUX_SC1);
}

}

extern "C" void kernel_launch(void* const* d_in, const int* in_sizes, int n_in, void* d_out, int out_size, void* d_ws,
                              size_t ws_size, hipStream_t stream) {
  Params p;
  for (int i = 0; i < n_in; ++i) printf("in[%d] = %p size %d\n", i, d_in[i], in_sizes[i]);
  printf("out %p ws %p\n", d_out, d_ws);
  for (int br = 0; br < 2; ++br) {
    p.x[br] = d_in[5 * br + 0];
    p.k[br] = d_in[5 * br + 1];
    p.v[br] = d_in[5 * br + 2];
    p.wq[br] = d_in[5 * br + 3];
    p.wo[br] = d_in[5 * br + 4];
  }
  p.out = d_out;
  p.ws = d_ws;
  hipLaunchKernelGGL(fused_decode_kernel, dim3(NWG), dim3(NTHR), 0, stream, p);
}
```

```cpp
#define PRE 0
#define POST 1
#define PRE_G 1
#define PRE_T 64
#define PRE_VGPR 0
#include <hip/hip_runtime.h>
#include <hip/hip_bf16.h>
#include <stdint.h>

namespace {

typedef unsigned v4u __attribute__((ext_vector_type(4)));
typedef unsigned v2u __attribute__((ext_vector_type(2)));
typedef float f32x4 __attribute__((ext_vector_type(4)));
typedef float f32x2 __attribute__((ext_vector_type(2)));
typedef short bf16x8 __attribute__((ext_vector_type(8)));
typedef __bf16 bf16x2 __attribute__((ext_vector_type(2)));

constexpr int NB = 2, DM = 512, NH = 32, SK = 8192, DH = 128, WIN = 2048;
constexpr int NWG = 256, NTHR = 512, NWAVE = 8;
constexpr int KEYS_WG = WIN / 2, KEYS_WAVE = KEYS_WG / NWAVE;
constexpr int NKV = KEYS_WAVE / 4;
constexpr int NW = 16;

constexpr int WS_EPOCH = 0;
constexpr int WS_ML = 4096;
constexpr int WS_BOX = 65536;
constexpr int WS_BYTES = WS_BOX + 4 * 64 * 64 * 64;

#define AUX_KV 2
#define AUX_W 2
#define BAR_A 1
#define WO_EARLY 8
#define WO_SPREAD 8
#define ML_AT 7
constexpr int AUX_SC1 = 16;
constexpr int RSRC_W3 = 0x00020000;
constexpr unsigned SPIN_CAP = 1u << 20;

struct Params {
  const void* x[2];
  const void* k[2];
  const void* v[2];
  const void* wq[2];
  const void* wo[2];
  void* out;
  void* ws;
};

__device__ __forceinline__ f32x4 mfma(v4u a, v4u b, f32x4 c) {
  return __builtin_amdgcn_mfma_f32_16x16x32_bf16(__builtin_bit_cast(bf16x8, a), __builtin_bit_cast(bf16x8, b), c, 0, 0, 0);
}
template <int CTRL> __device__ __forceinline__ unsigned dppu(unsigned x) {
  return (unsigned)__builtin_amdgcn_mov_dpp((int)x, CTRL, 0xf, 0xf, true);
}
template <int CTRL> __device__ __forceinline__ float dppf(float x) {
  return __builtin_bit_cast(float, __builtin_amdgcn_mov_dpp(__builtin_bit_cast(int, x), CTRL, 0xf, 0xf, true));
}
constexpr int ROR1 = 0x121, ROR2 = 0x122, ROR4 = 0x124, ROR8 = 0x128, BCAST = 0x150;
__device__ __forceinline__ unsigned pack_bf16(float lo, float hi) {
  bf16x2 r = {(__bf16)lo, (__bf16)hi};
  return __builtin_bit_cast(unsigned, r);
}
__device__ __forceinline__ float round_bf16(float x) { return __uint_as_float(pack_bf16(0.f, x) & 0xffff0000u); }
__device__ __forceinline__ float xrow_max(float x) {
  auto s = __builtin_amdgcn_permlane16_swap(__float_as_uint(x), __float_as_uint(x), false, false);
  x = fmaxf(__uint_as_float(s[0]), __uint_as_float(s[1]));
  auto t = __builtin_amdgcn_permlane32_swap(__float_as_uint(x), __float_as_uint(x), false, false);
  return fmaxf(__uint_as_float(t[0]), __uint_as_float(t[1]));
}
__device__ __forceinline__ float xrow_sum(float x) {
  auto s = __builtin_amdgcn_permlane16_swap(__float_as_uint(x), __float_as_uint(x), false, false);
  x = __uint_as_float(s[0]) + __uint_as_float(s[1]);
  auto t = __builtin_amdgcn_permlane32_swap(__float_as_uint(x), __float_as_uint(x), false, false);
  return __uint_as_float(t[0]) + __uint_as_float(t[1]);
}
__device__ __forceinline__ v4u spread(unsigned x, const unsigned (&sel)[4]) {
  v4u b;
  b.x = __builtin_amdgcn_perm(0u, x, sel[0]);
  b.y = __builtin_amdgcn_perm(0u, x, sel[1]);
  b.z = __builtin_amdgcn_perm(0u, x, sel[2]);
  b.w = __builtin_amdgcn_perm(0u, x, sel[3]);
  return b;
}
#define SCHED() __builtin_amdgcn_sched_barrier(0)
#define PROF_STAMPS
#define PROF_NS 8
#define PROF_OFF (4 << 20)
#define STAMP(i) do { SCHED(); asm volatile("s_memrealtime %0\n\ts_waitcnt lgkmcnt(0)" : "=s"(stamp[i]) :: "memory"); SCHED(); } while (0)
#define STAMP_OUT() do { if (lane == 0) { unsigned long long* d = (unsigned long long*)((char*)p.ws + PROF_OFF) + (size_t)(bid * 8 + w) * PROF_NS; for (int i_ = 0; i_ < PROF_NS; ++i_) d[i_] = stamp[i_]; } } while (0)
__device__ __forceinline__ void wg_barrier() {
  asm volatile("s_waitcnt lgkmcnt(0)\n\ts_barrier" ::: "memory");
}

__global__ void __launch_bounds__(NTHR, 2) fused_decode_kernel(Params p) {
  __shared__ __attribute__((aligned(16))) float lds[2 * NWAVE * DH + NWAVE];
  unsigned long long stamp[PROF_NS] = {0, 0, 0, 0, 0, 0, 0, 0};
  STAMP(0);
  const int tid = threadIdx.x, lane = tid & 63, w = __builtin_amdgcn_readfirstlane(tid >> 6);
  const int g = lane >> 4, n = lane & 15;
  const int bid = blockIdx.x, jj = bid >> 3, grp = (bid & 7) + 8 * (jj >> 2);
  const int br = grp >> 5, head = grp & 31, b = (jj >> 1) & 1, half = jj & 1;
  const int G = br * 2 + b, me = head * 2 + half;

  const size_t kv_off = ((size_t)(b * NH + head) * SK + (SK - WIN) + half * KEYS_WG) * (DH * 2);
  const __amdgpu_buffer_rsrc_t rK = __builtin_amdgcn_make_buffer_rsrc((char*)p.k[br] + kv_off, 0, KEYS_WG * DH * 2, RSRC_W3);
  const __amdgpu_buffer_rsrc_t rV = __builtin_amdgcn_make_buffer_rsrc((char*)p.v[br] + kv_off, 0, KEYS_WG * DH * 2, RSRC_W3);
  const __amdgpu_buffer_rsrc_t rQ = __builtin_amdgcn_make_buffer_rsrc((char*)p.wq[br], 0, DM * NH * DH * 2, RSRC_W3);
  const __amdgpu_buffer_rsrc_t rO = __builtin_amdgcn_make_buffer_rsrc((char*)p.wo[br] + (size_t)head * DH * DM * 2, 0, DH * DM * 2, RSRC_W3);
  const __amdgpu_buffer_rsrc_t rX = __builtin_amdgcn_make_buffer_rsrc((char*)p.x[br] + b * DM * 2, 0, DM * 2, RSRC_W3);
  const __amdgpu_buffer_rsrc_t rS = __builtin_amdgcn_make_buffer_rsrc((char*)p.ws, 0, WS_BYTES, RSRC_W3);

  v4u xr[2];
  xr[0] = __builtin_amdgcn_raw_buffer_load_b128(rX, (64 * w + 16 * g) * 2, 0, 0);
  xr[1] = __builtin_amdgcn_raw_buffer_load_b128(rX, (64 * w + 16 * g) * 2 + 16, 0, 0);
  const unsigned epoch = __builtin_amdgcn_raw_buffer_load_b32(rS, WS_EPOCH + G * 256, 0, AUX_SC1);
  v4u wr[NW];
  v4u kv[NKV];
  const int kvo = w * 4096 + (n >> 2) * 256 + g * 64 + (n & 3) * 16;
#define LD_K(t) kv[t] = __builtin_amdgcn_raw_buffer_load_b128(rK, kvo + ((t) & 3) * 1024, ((t) >> 2) * 32768, AUX_KV)
  {
    const int vo = (64 * w + 16 * g) * (NH * DH * 2) + head * (DH * 2) + n * 16;
#pragma unroll
    for (int t = 0; t < NW; ++t) {
      wr[t] = __builtin_amdgcn_raw_buffer_load_b128(rQ, vo, t * (NH * DH * 2), AUX_W);
      LD_K(t);
      SCHED();
    }
  }
  asm volatile("s_barrier" ::: "memory");
  SCHED();
  STAMP(1);

  unsigned sel0[4], sel1[4], selp[4];
#pragma unroll
  for (int i = 0; i < 4; ++i) {
    const bool here = ((n & 7) >> 1) == i;
    const unsigned lo = (n & 1) ? 0x01000c0cu : 0x0c0c0100u, hi = (n & 1) ? 0x03020c0cu : 0x0c0c0302u;
    sel0[i] = (here && n < 8) ? lo : 0x0c0c0c0cu;
    sel1[i] = (here && n < 8) ? hi : 0x0c0c0c0cu;
    selp[i] = here ? (n < 8 ? lo : hi) : 0x0c0c0c0cu;
  }

  {
    f32x4 acc = {0.f, 0.f, 0.f, 0.f};
#pragma unroll
    for (int t = 0; t < NW; ++t) {
      const unsigned xd = xr[t >> 3][(t >> 1) & 3];
      acc = mfma(wr[t], (t & 1) ? spread(xd, sel1) : spread(xd, sel0), acc);
    }
    if (n < 8) {
#pragma unroll
      for (int i = 0; i < 4; ++i) lds[w * DH + 32 * g + 8 * i + n] = acc[i];
    }
  }
  SCHED();
  STAMP(2);
  const int woo = (32 * g + (n >> 3)) * (DM * 2) + w * 128 + (n & 7) * 16;
#define LD_O(t) wr[t] = __builtin_amdgcn_raw_buffer_load_b128(rO, woo + ((t) & 1) * 2048, ((t) >> 1) * 4096, AUX_W)
#pragma unroll
  for (int t = 0; t < NW; ++t) {
    if (t < WO_EARLY) LD_O(t);
    LD_K(NW + t);
    SCHED();
  }
  wg_barrier();
  v4u qb[4];
  {
    f32x4 q0 = {0.f, 0.f, 0.f, 0.f}, q1 = {0.f, 0.f, 0.f, 0.f};
    const float* src = lds + 32 * g + 8 * (n & 3);
#pragma unroll
    for (int ww = 0; ww < NWAVE; ++ww) {
      q0 += *(const f32x4*)(src + ww * DH);
      q1 += *(const f32x4*)(src + ww * DH + 4);
    }
    v4u q;
    q.x = pack_bf16(q0[0], q0[1]);
    q.y = pack_bf16(q0[2], q0[3]);
    q.z = pack_bf16(q1[0], q1[1]);
    q.w = pack_bf16(q1[2], q1[3]);
    const v4u z = {0u, 0u, 0u, 0u};
#pragma unroll
    for (int t = 0; t < 4; ++t) qb[t] = ((n >> 2) == t) ? q : z;
  }
  SCHED();

  STAMP(3);
  float s[NKV / 4];
#pragma unroll
  for (int u = 0; u < NKV / 4; ++u) {
    f32x4 d = {0.f, 0.f, 0.f, 0.f};
#pragma unroll
    for (int t = 0; t < 4; ++t) d = mfma(kv[4 * u + t], qb[t], d);
#pragma unroll
    for (int t = 0; t < 4; ++t) kv[4 * u + t] = __builtin_amdgcn_raw_buffer_load_b128(rV, w * 4096 + lane * 16 + t * 1024, u * 32768, AUX_KV);
    const float raw = (dppf<0x00>(d[0]) + dppf<0x55>(d[1])) + (dppf<0xAA>(d[2]) + dppf<0xFF>(d[3]));
    s[u] = round_bf16(round_bf16(raw) * 0.088388347648318440f);
    SCHED();
  }
  constexpr float C2 = 1.4426950408889634f;
  float m = s[0];
#pragma unroll
  for (int u = 1; u < NKV / 4; ++u) m = fmaxf(m, s[u]);
  m = fmaxf(m, dppf<ROR4>(m));
  m = fmaxf(m, dppf<ROR8>(m));
  m = xrow_max(m) * C2;
  float l = 0.f;
  unsigned pk[NKV / 4];
#pragma unroll
  for (int u = 0; u < NKV / 4; ++u) {
    const float e = __builtin_amdgcn_exp2f(fmaf(s[u], C2, -m));
    l += e;
    const float hi = __uint_as_float(pack_bf16(e, e) & 0xffff0000u);
    pk[u] = pack_bf16(e, e - hi);
  }
  l += dppf<ROR4>(l);
  l += dppf<ROR8>(l);
  l = xrow_sum(l);
  const unsigned tag = epoch + 1u;
  const int ml_off = WS_ML + (G * NH + head) * 256;
  if (lane == 0) {
    const v4u rec = {__float_as_uint(m), tag, __float_as_uint(l), tag};
    __builtin_amdgcn_raw_buffer_store_b128(rec, rS, ml_off + (half * NWAVE + w) * 16, 0, AUX_SC1);
  }
  SCHED();

  if (lane == 0) lds[2 * NWAVE * DH + w] = m;
  wg_barrier();
  STAMP(4);
  float mwg;
  {
    const f32x4 m0 = *(const f32x4*)(lds + 2 * NWAVE * DH), m1 = *(const f32x4*)(lds + 2 * NWAVE * DH + 4);
    mwg = fmaxf(fmaxf(fmaxf(m0[0], m0[1]), fmaxf(m0[2], m0[3])), fmaxf(fmaxf(m1[0], m1[1]), fmaxf(m1[2], m1[3])));
  }
  const float fw = __builtin_amdgcn_exp2f(m - mwg);
  f32x4 o = {0.f, 0.f, 0.f, 0.f};
  v4u ml;
#pragma unroll
  for (int u = 0; u < NKV / 4; ++u) {
#pragma unroll
    for (int t = WO_EARLY + (u * (NW - WO_EARLY)) / WO_SPREAD; t < WO_EARLY + ((u + 1) * (NW - WO_EARLY)) / WO_SPREAD && t < NW; ++t) LD_O(t);
    if (u == ML_AT) ml = __builtin_amdgcn_raw_buffer_load_b128(rS, ml_off + n * 16, 0, AUX_SC1);
    SCHED();
    o = mfma(kv[4 * u + 0], spread(dppu<BCAST + 0>(pk[u]), selp), o);
    o = mfma(kv[4 * u + 1], spread(dppu<BCAST + 4>(pk[u]), selp), o);
    o = mfma(kv[4 * u + 2], spread(dppu<BCAST + 8>(pk[u]), selp), o);
    o = mfma(kv[4 * u + 3], spread(dppu<BCAST + 12>(pk[u]), selp), o);
    SCHED();
  }
  STAMP(5);
  {
    float* dst = lds + NWAVE * DH + w * DH + 32 * g + (n & 7) + (n >> 3) * 16;
    const float t0 = (o[0] + dppf<ROR8>(o[0])) * fw, t1 = (o[1] + dppf<ROR8>(o[1])) * fw;
    const float t2 = (o[2] + dppf<ROR8>(o[2])) * fw, t3 = (o[3] + dppf<ROR8>(o[3])) * fw;
    dst[0] = n < 8 ? t0 : t2;
    dst[8] = n < 8 ? t1 : t3;
  }
  wg_barrier();
  unsigned ox;
  {
    f32x2 a = {0.f, 0.f};
#pragma unroll
    for (int ww = 0; ww < NWAVE; ++ww) a += *(const f32x2*)(lds + NWAVE * DH + ww * DH + 2 * lane);
    ox = pack_bf16(a[0], a[1]);
  }
  f32x4 y = {0.f, 0.f, 0.f, 0.f};
#define WO_STEP(t) y = mfma(wr[t], spread(dppu<BCAST + (t)>(ox), selp), y);
  WO_STEP(0) WO_STEP(1) WO_STEP(2) WO_STEP(3) WO_STEP(4) WO_STEP(5) WO_STEP(6) WO_STEP(7)
  WO_STEP(8) WO_STEP(9) WO_STEP(10) WO_STEP(11) WO_STEP(12) WO_STEP(13) WO_STEP(14) WO_STEP(15)
#undef WO_STEP
  for (unsigned spins = 0; !__all(ml.y == tag && ml.w == tag); ++spins) {
    if (spins > SPIN_CAP) break;
    __builtin_amdgcn_s_sleep(1);
    asm volatile("" ::: "memory");
    ml = __builtin_amdgcn_raw_buffer_load_b128(rS, ml_off + n * 16, 0, AUX_SC1);
  }
  float fin;
  {
    const float mi = __uint_as_float(ml.x);
    float M = fmaxf(mi, dppf<ROR1>(mi));
    M = fmaxf(M, dppf<ROR2>(M));
    M = fmaxf(M, dppf<ROR4>(M));
    M = fmaxf(M, dppf<ROR8>(M));
    float L = __uint_as_float(ml.z) * __builtin_amdgcn_exp2f(mi - M);
    L += dppf<0xB1>(L);
    L += dppf<0x4E>(L);
    L += dppf<0x141>(L);
    L += dppf<0x140>(L);
    fin = __builtin_amdgcn_exp2f(mwg - M) / L;
  }
  {
    const bool own = (g >> 1) == (n >> 3), up = n >= 8;
    float z[4];
#pragma unroll
    for (int i = 0; i < 4; ++i) { const float r = dppf<ROR8>(y[i]); z[i] = own ? y[i] : r; }
    const float A = up ? z[1] : z[0], B = up ? z[3] : z[2];
    auto t = __builtin_amdgcn_permlane32_swap(__float_as_uint(A), __float_as_uint(B), false, false);
    const float val = (__uint_as_float(t[0]) + __uint_as_float(t[1])) * fin;
    const int cons = 8 * w + 4 * (g & 1) + 2 * (g >> 1) + (n >> 3);
    const v2u gr = {__float_as_uint(val), tag};
    __builtin_amdgcn_raw_buffer_store_b64(gr, rS, WS_BOX + ((G * 64 + cons) * 64 + me) * 64 + (n & 7) * 8, 0, AUX_SC1);
  }
  STAMP(6);
  if (w != 0) { return; }

  const int box = WS_BOX + (G * 64 + me) * 4096 + lane * 16;
  v4u r[4];
  for (unsigned spins = 0;; ++spins) {
    bool ok = true;
    asm volatile("" ::: "memory");
#pragma unroll
    for (int j = 0; j < 4; ++j) r[j] = __builtin_amdgcn_raw_buffer_load_b128(rS, box + j * 1024, 0, AUX_SC1);
#pragma unroll
    for (int j = 0; j < 4; ++j) ok = ok && r[j].y == tag && r[j].w == tag;
    if (__all(ok) || spins > SPIN_CAP) break;
    __builtin_amdgcn_s_sleep(1);
  }
  float a0 = (__uint_as_float(r[0].x) + __uint_as_float(r[1].x)) + (__uint_as_float(r[2].x) + __uint_as_float(r[3].x));
  float a1 = (__uint_as_float(r[0].z) + __uint_as_float(r[1].z)) + (__uint_as_float(r[2].z) + __uint_as_float(r[3].z));
  a0 += dppf<ROR4>(a0); a1 += dppf<ROR4>(a1);
  a0 += dppf<ROR8>(a0); a1 += dppf<ROR8>(a1);
  a0 = xrow_sum(a0); a1 = xrow_sum(a1);
  if (lane < 4) ((unsigned*)p.out)[(b * (2 * DM) + br * DM + me * 8) / 2 + lane] = pack_bf16(a0, a1);
  STAMP(7);
  if (bid == 0) {
    volatile unsigned long long* tw = (volatile unsigned long long*)((char*)p.ws + PROF_OFF);
    if (lane == 0) *tw = stamp[0];
  }
  if (me == 0 && lane == 0) __builtin_amdgcn_raw_buffer_store_b32(tag, rS, WS_EPOCH + G * 256, 0, AUX_SC1);
}

__global__ void __launch_bounds__(512) pre_kernel(unsigned long long* tw) {
  unsigned long long t;
  asm volatile("s_memrealtime %0\n\ts_waitcnt lgkmcnt(0)" : "=s"(t) :: "memory");
  if (blockIdx.x == 0 && threadIdx.x == 0) *tw = t;
}
__global__ void __launch_bounds__(512) post_kernel(unsigned long long* tw) {
  if (blockIdx.x == 0) {
    unsigned long long tgt = *(volatile unsigned long long*)tw + 8000, now;
    do { asm volatile("s_memrealtime %0\n\ts_waitcnt lgkmcnt(0)" : "=s"(now) :: "memory"); } while (now < tgt && now + 100000 > tgt);
  }
}
}

extern "C" void kernel_launch(void* const* d_in, const int* in_sizes, int n_in, void* d_out, int out_size, void* d_ws,
                              size_t ws_size, hipStream_t stream) {
  Params p;
  for (int br = 0; br < 2; ++br) {
    p.x[br] = d_in[5 * br + 0];
    p.k[br] = d_in[5 * br + 1];
    p.v[br] = d_in[5 * br + 2];
    p.wq[br] = d_in[5 * br + 3];
    p.wo[br] = d_in[5 * br + 4];
  }
  p.out = d_out;
  p.ws = d_ws;
  hipLaunchKernelGGL(fused_decode_kernel, dim3(NWG), dim3(NTHR), 0, stream, p);
  hipLaunchKernelGGL(post_kernel, dim3(1), dim3(64), 0, stream, (unsigned long long*)((char*)d_ws + PROF_OFF));
}
```

```cpp
#include <hip/hip_runtime.h>
#include <hip/hip_bf16.h>
#include <stdint.h>
#include <utility>

namespace {

typedef unsigned v4u __attribute__((ext_vector_type(4)));
typedef unsigned v2u __attribute__((ext_vector_type(2)));
typedef unsigned v8u __attribute__((ext_vector_type(8)));
typedef unsigned v16u __attribute__((ext_vector_type(16)));
typedef float f32x4 __attribute__((ext_vector_type(4)));
typedef float f32x2 __attribute__((ext_vector_type(2)));
typedef short s16x8 __attribute__((ext_vector_type(8)));
typedef __bf16 bf16x2 __attribute__((ext_vector_type(2)));
typedef __bf16 bf16x8 __attribute__((ext_vector_type(8)));
typedef __bf16 bf16x16 __attribute__((ext_vector_type(16)));

constexpr int DM = 512, NH = 32, SK = 8192, DH = 128, WIN = 2048;
constexpr int NWG = 256, NWAVE = 8, NTHR = (NWAVE + 1) * 64;
constexpr int NKV = 32;
constexpr int NQ = 4;
constexpr int NW = 16;

constexpr int WS_EPOCH = 0;
constexpr int WS_SEEN = 512;
constexpr int WS_POST = 1024;
constexpr int WS_BOX = 16384;
constexpr int WS_QBOX = WS_BOX + 4 * 64 * 64 * 64;
constexpr int WS_BYTES = WS_QBOX + 64 * 2 * 4 * 1024;

constexpr int W_USE = 8;
constexpr int W_FIRST = 5, W_BAR = 2, W_FIX = 4;
constexpr int Q_LAG = 6, QB_AT = 30, WO_FROM = 20, V_TAIL = 8;
constexpr int TAU_FAST = 1755, TAU_OPEN = 1820, TAU_SLOW = 1970;
constexpr int WL0 = 2, WL1 = 3, WL2 = 4, WL3 = 5;
constexpr int TH1 = -40, TH2 = 0, TH3 = 40;
constexpr int EASE = 20, EASE_N = 10;
constexpr int PACE_FROM = 9, LN_EVERY = 3, ORG0 = 14, MARG0 = 27;
constexpr int NCP = 5, CP_POST[NCP] = {10, 30, 42, 56, 67}, CP_BIAS[NCP] = {0, 0, 0, 9, 0};
constexpr int DL0 = 420, RD0 = 200, DL1 = 115, RD1 = 70, LATE = 30;
constexpr int LAG_LO = -20, LAG_MID = -12, LAG_A = -21, LAG_B = -11, LAG_Z = -4, LAG_FP = -42, LAG_FAST = -29, AT_SLOW = -25;
constexpr int Q_WAIT = 100, Q_AT = 500;
constexpr int POLL_LAG = 102, POLL_MIN = 40, POLL_MAX = 200;
constexpr int CODE_LEN = 17100;

constexpr int AUX_NT = 2, AUX_SC1 = 16;
constexpr int RSRC_W3 = 0x00020000;
constexpr unsigned SPIN_CAP = 1u << 20;

constexpr int NSEQ = 2 * NKV + NQ + NW, WO_TO = NSEQ - V_TAIL;
constexpr int cmax(int a, int b) { return a > b ? a : b; }
struct Plan {
  int kind[NSEQ], idx[NSEQ], uniq[NSEQ + 1], ops[NSEQ + 1], pos[4][NKV], cnt[4];
  int qp[NSEQ], sc[NSEQ], tr[NSEQ], pv0[NSEQ + 1];
  int at_sm, at_mb;
};
constexpr Plan make_plan() {
  Plan P{};
  for (int s = 0; s < NSEQ; ++s) {
    int k = 1;
    if (s < NQ) k = 0;
    else if (s < NQ + 4) k = 2;
    else if (s >= WO_FROM && s < WO_TO && ((s - WO_FROM + 1) * (NW - 4)) / (WO_TO - WO_FROM) != ((s - WO_FROM) * (NW - 4)) / (WO_TO - WO_FROM)) k = 2;
    else if (P.cnt[1] == NKV) k = 3;
    P.kind[s] = k; P.idx[s] = P.cnt[k]; P.pos[k][P.cnt[k]++] = s;
    P.uniq[s + 1] = P.uniq[s] + (k == 2 && P.idx[s] >= 4 ? 0 : 4);
    P.ops[s + 1] = P.ops[s] + (k == 2 ? 2 : 1);
    P.qp[s] = -1; P.sc[s] = -1; P.tr[s] = -1;
  }
  for (int t = 0; t < NQ; ++t) P.qp[P.pos[0][t] + Q_LAG] = t;
  for (int t = 0; t < NW; ++t) P.tr[P.pos[2][t] + W_USE] = t;
  int at = QB_AT;
  for (int u = 0; u < NKV / 4; ++u) { at = cmax(at + 1, P.pos[1][4 * u + 3] + W_USE); P.sc[at] = u; }
  P.at_sm = at + 1;
  P.at_mb = at + 16;
  int nv = 0;
  for (int s = 0; s < NSEQ; ++s) {
    P.pv0[s] = nv;
    if (s > P.at_sm) { int c = 0; while (nv < NKV && P.pos[3][nv] + (s >= NSEQ - 6 ? WL3 : W_USE) <= s && c < 2) { ++nv; ++c; } }
  }
  P.pv0[NSEQ] = nv;
  return P;
}
constexpr Plan PL = make_plan();
static_assert(PL.cnt[0] == NQ && PL.cnt[1] == NKV && PL.cnt[2] == NW && PL.cnt[3] == NKV && PL.at_mb < NSEQ, "sequence");
constexpr bool rides(int s) { return PL.uniq[s + 1] == PL.uniq[s]; }
constexpr int inflight(int s, int tiles) {
  int j = s, c = 0;
  while (j > 0 && c < tiles) { --j; if (!rides(j)) ++c; }
  return c < tiles ? 63 : PL.ops[s] - PL.ops[j + 1];
}
constexpr int cp_at(int s) { for (int k = 0; k < NCP; ++k) if (CP_POST[k] == s) return k; return -1; }

struct Branch { const void *k, *wq, *v, *wo, *x; void *out, *ws, *unused; };
struct alignas(64) Params { Branch br[2]; };
static_assert(sizeof(Params) == 128, "no padding");

template <class F, int... I> __device__ __forceinline__ void sfor_(F f, std::integer_sequence<int, I...>) { (f(std::integral_constant<int, I>{}), ...); }
template <int N, class F> __device__ __forceinline__ void sfor(F f) { sfor_(f, std::make_integer_sequence<int, N>{}); }

__device__ __forceinline__ f32x4 mfma(v4u a, v4u b, f32x4 c) {
  return __builtin_amdgcn_mfma_f32_16x16x32_bf16(__builtin_bit_cast(s16x8, a), __builtin_bit_cast(s16x8, b), c, 0, 0, 0);
}
__device__ __forceinline__ f32x4 mfma_pair(v4u a, v8u b, f32x4 c, int sel) {
  return __builtin_amdgcn_smfmac_f32_16x16x64_bf16(__builtin_bit_cast(bf16x8, a), __builtin_bit_cast(bf16x16, b), c, sel, 0, 0);
}
template <int CTRL> __device__ __forceinline__ unsigned dppu(unsigned x) {
  return (unsigned)__builtin_amdgcn_mov_dpp((int)x, CTRL, 0xf, 0xf, true);
}
template <int CTRL> __device__ __forceinline__ float dppf(float x) {
  return __builtin_bit_cast(float, __builtin_amdgcn_mov_dpp(__builtin_bit_cast(int, x), CTRL, 0xf, 0xf, true));
}
constexpr int XOR1 = 0xB1, XOR2 = 0x4E, HMIR = 0x141, ROR4 = 0x124, ROR8 = 0x128, BCAST = 0x150;
__device__ __forceinline__ unsigned pack_bf16(float lo, float hi) {
  bf16x2 r = {(__bf16)lo, (__bf16)hi};
  return __builtin_bit_cast(unsigned, r);
}
__device__ __forceinline__ float round_bf16(float x) { return __uint_as_float(pack_bf16(0.f, x) & 0xffff0000u); }
__device__ __forceinline__ float swap16_sum(float a, float b) {
  auto s = __builtin_amdgcn_permlane16_swap(__float_as_uint(a), __float_as_uint(b), false, false);
  return __uint_as_float(s[0]) + __uint_as_float(s[1]);
}
__device__ __forceinline__ float swap32_sum(float a, float b) {
  auto s = __builtin_amdgcn_permlane32_swap(__float_as_uint(a), __float_as_uint(b), false, false);
  return __uint_as_float(s[0]) + __uint_as_float(s[1]);
}
__device__ __forceinline__ float xrow_max(float x) {
  auto s = __builtin_amdgcn_permlane16_swap(__float_as_uint(x), __float_as_uint(x), false, false);
  x = fmaxf(__uint_as_float(s[0]), __uint_as_float(s[1]));
  auto t = __builtin_amdgcn_permlane32_swap(__float_as_uint(x), __float_as_uint(x), false, false);
  return fmaxf(__uint_as_float(t[0]), __uint_as_float(t[1]));
}
__device__ __forceinline__ __amdgpu_buffer_rsrc_t rsrc(unsigned lo, unsigned hi, int bytes) {
  return __builtin_amdgcn_make_buffer_rsrc((void*)(((unsigned long long)hi << 32) | lo), 0, bytes, RSRC_W3);
}
__device__ __forceinline__ unsigned now32() { return (unsigned)__builtin_amdgcn_s_memrealtime(); }
__device__ __forceinline__ void pause_until(unsigned due) {
  for (int d = (int)(now32() - due); d < 0 && d > -800; d = (int)(now32() - due)) __builtin_amdgcn_s_sleep(1);
}
#define SCHED() __builtin_amdgcn_sched_barrier(0)
#define PIN(x) asm volatile("" : "+v"(x))
#define FENCE() asm volatile("" ::: "memory")
__device__ __forceinline__ void wg_barrier() {
  asm volatile("s_waitcnt lgkmcnt(0)\n\ts_barrier" ::: "memory");
}
template <int N> __device__ __forceinline__ void wait_vm() {
  if constexpr (N < 63) asm volatile("s_waitcnt vmcnt(%0)" :: "n"(N) : "memory");
}
template <int C0, int C1, int C2_, int C3, int U, int X> __device__ __forceinline__ void throttle(unsigned org, unsigned tau) {
  asm volatile("s_memrealtime vcc\n\ts_waitcnt lgkmcnt(0)\n\ts_mul_i32 vcc_hi, %1, %6\n\ts_sub_u32 vcc_lo, vcc_lo, %0\n\t"
               "s_lshr_b32 vcc_hi, vcc_hi, 8\n\ts_sub_u32 vcc_lo, vcc_lo, vcc_hi\n\t"
               "s_cmpk_lt_i32 vcc_lo, %9\n\ts_cbranch_scc0 3f\n\t"
               "s_cmpk_lt_i32 vcc_lo, %8\n\ts_cbranch_scc0 2f\n\t"
               "s_cmpk_lt_i32 vcc_lo, %7\n\ts_cbranch_scc0 1f\n\t"
               "s_waitcnt vmcnt(%2)\n1:\n\ts_waitcnt vmcnt(%3)\n2:\n\ts_waitcnt vmcnt(%4)\n3:\n\ts_waitcnt vmcnt(%5)"
               :: "s"(org), "s"(tau), "n"(C0), "n"(C1), "n"(C2_), "n"(C3), "n"(U), "n"(TH1 + X), "n"(TH2 + X), "n"(TH3 + X)
               : "memory", "scc", "vcc");
}
__device__ __forceinline__ void arrive(char* lds, int off, unsigned v) {
  FENCE();
  if ((threadIdx.x & 63) == 0) __hip_atomic_fetch_add((unsigned*)__builtin_assume_aligned(lds + off, 4), v, __ATOMIC_RELAXED, __HIP_MEMORY_SCOPE_WORKGROUP);
  FENCE();
}
__device__ __forceinline__ unsigned await(char* lds, int off, unsigned mask, unsigned want) {
  unsigned v;
  FENCE();
  while (((v = __builtin_amdgcn_readfirstlane(__hip_atomic_load((unsigned*)__builtin_assume_aligned(lds + off, 4), __ATOMIC_RELAXED, __HIP_MEMORY_SCOPE_WORKGROUP))) & mask) != want)
    __builtin_amdgcn_s_sleep(1);
  FENCE();
  return v;
}
__device__ __forceinline__ unsigned mean_post(v2u post, unsigned tag, unsigned late) {
  int d = post.y == tag ? (int)(post.x - late) : 0;
  d += (int)dppu<XOR1>(d); d += (int)dppu<XOR2>(d); d += (int)dppu<HMIR>(d); d += (int)dppu<ROR8>(d);
  auto s = __builtin_amdgcn_permlane16_swap(d, d, false, false);
  d = s[0] + s[1];
  auto t = __builtin_amdgcn_permlane32_swap(d, d, false, false);
  return late + ((int)(t[0] + t[1]) >> 6);
}

constexpr int LDS_Q = 0;
constexpr int LDS_QF = NWAVE * 2 * DH * 4;
constexpr int LDS_ML = LDS_QF + DH * 2;
constexpr int LDS_O = LDS_ML + NWAVE * 8, O_STRIDE = DH * 2 + 16;
constexpr int LDS_LINE = LDS_O + NWAVE * O_STRIDE;
constexpr int LDS_CNT = LDS_LINE + 16;
constexpr int LDS_BYTES = LDS_CNT + 48;

__global__ void __launch_bounds__(NTHR, 3) fused_decode_kernel(Params p) {
  __shared__ __attribute__((aligned(16))) char lds[LDS_BYTES];
  const int bid = blockIdx.x, jj = bid >> 3;
  v16u ka;
  asm volatile("s_load_dwordx16 %0, %1, %2" : "=s"(ka) : "s"(__builtin_amdgcn_kernarg_segment_ptr()), "s"((bid & 4) << 4));
  const int tid = threadIdx.x, lane = tid & 63, w = __builtin_amdgcn_readfirstlane(tid >> 6);
  const int g = lane >> 4, n = lane & 15;
  unsigned cw0 = 0, cw1 = 0, cw2 = 0;
  if (w == NWAVE) {
    unsigned long long pc;
    asm volatile("s_getpc_b64 %0" : "=s"(pc));
    const __amdgpu_buffer_rsrc_t rC = __builtin_amdgcn_make_buffer_rsrc((void*)(pc & ~127ull), 0, CODE_LEN, RSRC_W3);
    cw0 = __builtin_amdgcn_raw_buffer_load_b32(rC, lane * 128, 0, 0);
    cw1 = __builtin_amdgcn_raw_buffer_load_b32(rC, lane * 128 + 8192, 0, 0);
    cw2 = __builtin_amdgcn_raw_buffer_load_b32(rC, lane * 128 + 16384, 0, 0);
  }
  SCHED();
  const int br = (bid >> 2) & 1, head = (bid & 3) * 8 + (jj >> 2), b = (jj >> 1) & 1, half = jj & 1;
  const int G = br * 2 + b, GC = G ^ 2;
  const int me = head * 2 + half;

  const int qrow = 128 * (jj & 3) + 16 * w + 4 * g;
  int qo = w < NWAVE ? qrow * (NH * DH * 2) + head * (DH * 2) + n * 16 : DM * NH * DH * 2;
  asm volatile("s_waitcnt lgkmcnt(0)" : "+s"(ka), "+v"(qo));
  const __amdgpu_buffer_rsrc_t rK = rsrc(ka[0], ka[1], 2 * NH * SK * DH * 2);
  const __amdgpu_buffer_rsrc_t rQ = rsrc(ka[2], ka[3], DM * NH * DH * 2);
  const __amdgpu_buffer_rsrc_t rV = rsrc(ka[4], ka[5], 2 * NH * SK * DH * 2);
  const __amdgpu_buffer_rsrc_t rO = rsrc(ka[6], ka[7], NH * DH * DM * 2);
  const __amdgpu_buffer_rsrc_t rX = rsrc(ka[8], ka[9], 2 * DM * 2);
  const __amdgpu_buffer_rsrc_t rS = rsrc(ka[12], ka[13], WS_BYTES);
  const int kv_off = ((b * NH + head) * SK + (SK - WIN)) * (DH * 2) + half * 32768 + w * 4096;
  const int ko = kv_off + (n >> 2) * 256 + g * 64 + (n & 3) * 16;
  const int vo = kv_off + lane * 16;
  const int oo = (head * DH + 32 * (g >> 1) + (g & 1)) * (DM * 2) + w * 128 + n * 8;
#define KV_SOFF(i) (((i) >> 2) * 65536 + (((i) + head) & 3) * 1024)

  v4u wq[NQ], kk[NKV], vv[NKV], qb[4];
  v8u wt[4][2];
  v2u wo[2 * NW], xd;
  unsigned tag = 0, msk[4], pk[NKV / 4], hi0 = 0, hi1 = 0, org = 0, tau = TAU_OPEN, tb = 0;
  int pub_off = 0, ow_off = 0, ork[4] = {0, 0, 0, 0}, sel = 0;
  float s[NKV / 4], m = 0.f, fw = 0.f;
  f32x4 qa = {0.f, 0.f, 0.f, 0.f}, o = {0.f, 0.f, 0.f, 0.f};
  constexpr float C2 = 1.4426950408889634f;

  auto spread = [&](unsigned x) { return v4u{x & msk[0], x & msk[1], x & msk[2], x & msk[3]}; };
  auto pv = [&](auto T) {
    constexpr int t = T.value;
    o = mfma(vv[t], spread(dppu<BCAST + 4 * (t & 3)>(pk[t >> 2])), o);
    PIN(o);
  };

  auto slot = [&](auto S) {
    constexpr int sq = S.value, kind = PL.kind[sq], i = PL.idx[sq];
    if constexpr (sq < W_FIRST || rides(sq)) {
    } else if constexpr (sq <= PACE_FROM) wait_vm<inflight(sq, W_FIX) + (sq < W_FIRST + W_FIX ? 1 : 0)>();
    else throttle<inflight(sq, WL0), inflight(sq, WL1), inflight(sq, WL2), inflight(sq, WL3), PL.uniq[sq] - 4,
                  cmax(sq + 1 - (NSEQ - EASE_N), 0) * EASE / EASE_N>(org, tau);
    if constexpr (kind == 0) wq[i] = __builtin_amdgcn_raw_buffer_load_b128(rQ, qo, i * (NH * DH * 2), AUX_NT);
    if constexpr (kind == 1) kk[i] = __builtin_amdgcn_raw_buffer_load_b128(rK, ko, KV_SOFF(i), AUX_NT);
    if constexpr (kind == 2) {
      constexpr int kb = i >> 2, u = i & 3, r0 = 64 * (u >> 1) + 16 * (u & 1);
      const int kr = (kb + jj) & 3;
      wo[2 * i] = __builtin_amdgcn_raw_buffer_load_b64(rO, oo, (r0 + 2 * kr) * 1024, 0);
      wo[2 * i + 1] = __builtin_amdgcn_raw_buffer_load_b64(rO, oo, (r0 + 8 + 2 * kr) * 1024, 0);
    }
    if constexpr (kind == 3) vv[i] = __builtin_amdgcn_raw_buffer_load_b128(rV, vo, KV_SOFF(i), AUX_NT);
    SCHED();
    if constexpr (sq == W_FIRST - 1) {
      xd = __builtin_amdgcn_raw_buffer_load_b64(rX, ((n >> 3) * DM + qrow) * 2, 0, 0);
#pragma unroll
      for (int j = 0; j < 4; ++j) msk[j] = ((n & 7) >> 1) == j ? ((n & 1) ? 0xffff0000u : 0x0000ffffu) : 0u;
      tb = __builtin_amdgcn_readfirstlane(*(const unsigned*)(lds + LDS_LINE + 12));
    }
    if constexpr (cp_at(sq) >= 0) arrive(lds, LDS_CNT + 16 + 4 * cp_at(sq), ((now32() - tb) << 4) + 1u);
    if constexpr (sq >= PACE_FROM && (sq - PACE_FROM) % LN_EVERY == 0) {
      FENCE();
      const v2u ln = *(const v2u*)(lds + LDS_LINE);
      org = __builtin_amdgcn_readfirstlane(ln.x);
      tau = __builtin_amdgcn_readfirstlane(ln.y);
    }
    if constexpr (PL.qp[sq] >= 0) {
      constexpr int t = PL.qp[sq];
      qa = mfma(wq[t], spread(__builtin_amdgcn_perm(xd[t >> 1], xd[t >> 1], (t & 1) ? 0x03020302u : 0x01000100u)), qa);
      PIN(qa);
      if constexpr (t == NQ - 1) {
#pragma unroll
        for (int j = 0; j < 4; ++j) ((float*)(lds + LDS_Q))[(2 * w + (n >> 3)) * DH + 32 * g + 8 * j + (n & 7)] = qa[j];
        arrive(lds, LDS_CNT, 1u);
      }
    }
    if constexpr (sq == QB_AT) {
      await(lds, LDS_CNT + 4, ~0u, 1u);
      const v4u q = *(const v4u*)(lds + LDS_QF + (32 * g + 8 * (n & 3)) * 2);
      tag = __builtin_amdgcn_readfirstlane(*(const unsigned*)(lds + LDS_LINE + 8));
      const v4u z = {0u, 0u, 0u, 0u};
#pragma unroll
      for (int t = 0; t < 4; ++t) qb[t] = ((n >> 2) == t) ? q : z;
    }
    if constexpr (PL.sc[sq] >= 0) {
      constexpr int u = PL.sc[sq];
      f32x4 d = {0.f, 0.f, 0.f, 0.f};
#pragma unroll
      for (int t = 0; t < 4; ++t) d = mfma(kk[4 * u + t], qb[t], d);
      const float raw = (dppf<0x00>(d[0]) + dppf<0x55>(d[1])) + (dppf<0xAA>(d[2]) + dppf<0xFF>(d[3]));
      s[u] = round_bf16(round_bf16(raw) * 0.088388347648318440f);
      PIN(s[u]);
    }
    if constexpr (sq == PL.at_sm) {
      m = s[0];
#pragma unroll
      for (int u = 1; u < NKV / 4; ++u) m = fmaxf(m, s[u]);
      m = fmaxf(m, dppf<ROR4>(m));
      m = fmaxf(m, dppf<ROR8>(m));
      m = xrow_max(m) * C2;
      float l = 0.f;
#pragma unroll
      for (int u = 0; u < NKV / 4; ++u) {
        const float e = __builtin_amdgcn_exp2f(fmaf(s[u], C2, -m));
        l += e;
        pk[u] = pack_bf16(e, e);
        PIN(pk[u]);
      }
      l += dppf<ROR4>(l);
      l += dppf<ROR8>(l);
      l = swap16_sum(l, l);
      l = swap32_sum(l, l);
      if (lane == 0) *(f32x2*)(lds + LDS_ML + w * 8) = f32x2{m, l};
      arrive(lds, LDS_CNT + 8, 1u);
    }
    if constexpr (sq == PL.at_mb) {
      await(lds, LDS_CNT + 8, ~0u, NWAVE);
      const f32x2 r = *(const f32x2*)(lds + LDS_ML + (lane & 7) * 8);
      float M = fmaxf(r[0], dppf<XOR1>(r[0]));
      M = fmaxf(M, dppf<XOR2>(M));
      M = fmaxf(M, dppf<HMIR>(M));
      float L = r[1] * __builtin_amdgcn_exp2f(r[0] - M);
      PIN(L);
      L += dppf<XOR1>(L);
      L += dppf<XOR2>(L);
      L += dppf<HMIR>(L);
      fw = __builtin_amdgcn_exp2f(m - M) / L;
      const unsigned lse = __float_as_uint(M + __builtin_amdgcn_logf(L));
      hi0 = (tag << 16) | (lse & 0xffffu);
      hi1 = (tag & 0xffff0000u) | (lse >> 16);
      pub_off = lane < 32 ? WS_BOX + ((G * 64 + 8 * w + (n >> 1)) * 64 + me) * 64 + (2 * (n & 1) + g) * 16 : WS_BYTES;
      ow_off = LDS_O + w * O_STRIDE + g * 64 + n * 8;
#pragma unroll
      for (int kb = 0; kb < 4; ++kb) { ork[kb] = LDS_O + (2 * (n >> 2) + (n & 1)) * O_STRIDE + g * 64 + ((kb + jj) & 3) * 16; PIN(ork[kb]); }
      sel = (n & 2) ? 0xEEEE : 0x4444;
      PIN(fw); PIN(hi0); PIN(hi1); PIN(pub_off); PIN(ow_off); PIN(sel);
    }
    if constexpr (PL.pv0[sq + 1] > PL.pv0[sq]) pv(std::integral_constant<int, PL.pv0[sq]>{});
    if constexpr (PL.pv0[sq + 1] > PL.pv0[sq] + 1) pv(std::integral_constant<int, PL.pv0[sq] + 1>{});
    if constexpr (PL.tr[sq] >= 0) {
      constexpr int t = PL.tr[sq];
#pragma unroll
      for (int c = 0; c < 4; ++c) {
        unsigned r = __builtin_amdgcn_perm(wo[2 * t + 1][c >> 1], wo[2 * t][c >> 1], (c & 1) ? 0x07060302u : 0x05040100u);
        PIN(r);
        wt[t >> 2][c >> 1][2 * (t & 3) + (c & 1)] = r;
      }
    }
    SCHED();
  };

  sfor<W_BAR>(slot);
  if (w == NWAVE) {
    tb = now32();
    if (lane < 12) *(unsigned*)(lds + LDS_CNT + lane * 4) = 0u;
    if (lane == 0) *(v4u*)(lds + LDS_LINE) = v4u{tb + ORG0, TAU_OPEN, 0u, tb};
  }
  wg_barrier();

  if (__builtin_expect(w == NWAVE, 0)) {
    const __amdgpu_buffer_rsrc_t rY = rsrc(ka[10], ka[11], 2 * 2 * DM * 2);
    const int out_at = (b * (2 * DM) + (br ^ 1) * DM + me * 8) * 2, grp = br * NH + head;
    const unsigned epoch = __builtin_amdgcn_raw_buffer_load_b32(rS, WS_EPOCH, 0, AUX_SC1);
    unsigned touch = __builtin_amdgcn_raw_buffer_load_b32(rY, out_at, 0, 0);
    PIN(touch); PIN(cw0); PIN(cw1); PIN(cw2);
    tag = __builtin_amdgcn_readfirstlane(epoch) + 0x00010001u;
    org = tb + ORG0;
    unsigned dl = 0;
    int at = -MARG0;
    bool sped = false, slowed = false;
    auto post = [&](auto K) {
      constexpr int k = K.value;
      dl = k == 0 ? tb + DL0 : org + ((tau * PL.uniq[CP_POST[k]]) >> 8) + DL1;
      const unsigned tcp = tb + (await(lds, LDS_CNT + 16 + 4 * k, 15u, NWAVE) >> 7);
      if ((jj & 3) == (k & 3) && (int)(now32() - dl) < 0)
        __builtin_amdgcn_raw_buffer_store_b64(v2u{tcp, tag}, rS, lane == 0 ? WS_POST + k * 1024 + grp * 16 : WS_BYTES, 0, AUX_SC1);
    };
    auto line = [&](auto K) {
      constexpr int k = K.value, U = PL.uniq[CP_POST[k]];
      pause_until(dl + (k == 0 ? RD0 : RD1));
      FENCE();
      const unsigned tm = CP_BIAS[k] + __builtin_amdgcn_readfirstlane(
          mean_post(__builtin_amdgcn_raw_buffer_load_b64(rS, WS_POST + k * 1024 + lane * 16, 0, AUX_SC1), tag, dl + LATE));
      if constexpr (k > 0) {
        const int lag = (int)(tm - org - ((tau * U) >> 8));
        const int drift = ((lag - at) * (65536 / (U - PL.uniq[CP_POST[k - 1]]))) >> 8;
        int t2 = tau;
        if constexpr (k == 1) t2 = TAU_FAST + max(lag - LAG_A, 0) * 9;
        else if (lag > (k == NCP - 1 ? LAG_Z : LAG_B)) t2 += sped ? 0 : (drift * 6) >> 2;
        else if (slowed) t2 += lag < LAG_FP ? (drift * 3) >> 2 : 0;
        else if (lag < LAG_FAST) t2 -= 60;
        t2 = min(max(t2, TAU_FAST), TAU_SLOW);
        slowed = t2 > (int)tau;
        at = slowed ? AT_SLOW : t2 < (int)tau ? LAG_LO - (((int)tau - t2) >> 3) : min(max(lag, LAG_LO), LAG_MID);
        sped = k > 1 && t2 < (int)tau;
        tau = t2;
      }
      org = tm - at - ((tau * U) >> 8);
      if (lane == 0) *(v2u*)(lds + LDS_LINE) = v2u{org, tau};
      FENCE();
    };
    await(lds, LDS_CNT, ~0u, NWAVE);
    const int qbox = WS_QBOX + grp * 8192 + lane * 16;
#pragma unroll
    for (int bb = 0; bb < 2; ++bb) {
      f32x2 a[NWAVE];
#pragma unroll
      for (int ww = 0; ww < NWAVE; ++ww) a[ww] = *(const f32x2*)(lds + LDS_Q + ((2 * ww + bb) * DH + 2 * lane) * 4);
      const f32x2 t = ((a[0] + a[1]) + (a[2] + a[3])) + ((a[4] + a[5]) + (a[6] + a[7]));
      __builtin_amdgcn_raw_buffer_store_b128(v4u{__float_as_uint(t[0]), tag, __float_as_uint(t[1]), tag}, rS, qbox + bb * 4096 + (jj & 3) * 1024, 0, AUX_SC1);
    }
    const unsigned tq = now32();
    post(std::integral_constant<int, 0>{});
    pause_until(tq + Q_WAIT);
    pause_until(tb + Q_AT);
    v4u r[4];
    for (unsigned spins = 0;; ++spins) {
      FENCE();
#pragma unroll
      for (int j = 0; j < 4; ++j) r[j] = __builtin_amdgcn_raw_buffer_load_b128(rS, qbox + b * 4096, j * 1024, AUX_SC1);
      unsigned bad = 0;
#pragma unroll
      for (int j = 0; j < 4; ++j) bad |= (r[j].y ^ tag) | (r[j].w ^ tag);
      if (__all(bad == 0) || spins > SPIN_CAP) break;
      __builtin_amdgcn_s_sleep(8);
    }
    *(unsigned*)(lds + LDS_QF + lane * 4) = pack_bf16((__uint_as_float(r[0].x) + __uint_as_float(r[1].x)) + (__uint_as_float(r[2].x) + __uint_as_float(r[3].x)),
                                                      (__uint_as_float(r[0].z) + __uint_as_float(r[1].z)) + (__uint_as_float(r[2].z) + __uint_as_float(r[3].z)));
    if (lane == 0) *(unsigned*)(lds + LDS_LINE + 8) = tag;
    arrive(lds, LDS_CNT + 4, 1u);
    if ((jj & 3) == 1) __builtin_amdgcn_raw_buffer_store_b64(v2u{tag, ~tag}, rS, lane == 0 ? WS_SEEN + grp * 8 : WS_BYTES, 0, AUX_SC1);
    line(std::integral_constant<int, 0>{});
    sfor<NCP - 1>([&](auto K) {
      post(std::integral_constant<int, K.value + 1>{});
      if (K.value == NCP - 3 && bid == 0) {
#pragma unroll 1
        for (unsigned spins = 0; spins < SPIN_CAP; ++spins) {
          FENCE();
          const v2u t = __builtin_amdgcn_raw_buffer_load_b64(rS, WS_SEEN + lane * 8, 0, AUX_SC1);
          if (__all(t.x == tag && t.y == ~tag)) break;
          __builtin_amdgcn_s_sleep(8);
        }
      }
      line(std::integral_constant<int, K.value + 1>{});
    });
    int box = WS_BOX + (GC * 64 + me) * 4096 + (lane >> 2) * 128 + (lane & 3) * 16, out_off = lane == 0 ? out_at : 1 << 20;
    PIN(box); PIN(out_off);
    unsigned due = __builtin_amdgcn_readfirstlane(org + ((tau * PL.uniq[NSEQ]) >> 8) + POLL_LAG);
    asm volatile("" : "+s"(due));
    wg_barrier();
    if (bid == 0) __builtin_amdgcn_raw_buffer_store_b32(tag, rS, lane == 0 ? WS_EPOCH : WS_BYTES, 0, AUX_SC1);
    {
      const unsigned t = now32(), first = t + __builtin_amdgcn_readfirstlane(min(max((int)(due - t), POLL_MIN), POLL_MAX));
      while ((int)(now32() - first) < 0) {}
    }
    for (unsigned spins = 0;; ++spins) {
      FENCE();
#pragma unroll
      for (int j = 0; j < 4; ++j) r[j] = __builtin_amdgcn_raw_buffer_load_b128(rS, box, (j >> 1) * 2048 + (j & 1) * 64, AUX_SC1);
      unsigned bad = 0;
#pragma unroll
      for (int j = 0; j < 4; ++j) bad |= __builtin_amdgcn_perm(r[j].w, r[j].y, 0x07060302u) ^ tag;
      const bool done = __builtin_amdgcn_ballot_w64(bad != 0) == 0 || spins > SPIN_CAP;
      float a0 = 0.f, a1 = 0.f;
#pragma unroll
      for (int h = 0; h < 2; ++h) {
        const float l0 = __uint_as_float(__builtin_amdgcn_perm(r[2 * h].w, r[2 * h].y, 0x05040100u));
        const float l1 = __uint_as_float(__builtin_amdgcn_perm(r[2 * h + 1].w, r[2 * h + 1].y, 0x05040100u));
        const float w0 = __builtin_amdgcn_rcpf(1.f + __builtin_amdgcn_exp2f(l1 - l0));
        const float x1 = __uint_as_float(r[2 * h + 1].x), z1 = __uint_as_float(r[2 * h + 1].z);
        const float c0 = fmaf(w0, __uint_as_float(r[2 * h].x) - x1, x1), c1 = fmaf(w0, __uint_as_float(r[2 * h].z) - z1, z1);
        a0 = h ? a0 + c0 : c0;
        a1 = h ? a1 + c1 : c1;
      }
      float a = swap16_sum(a0, a1);
      a = swap32_sum(a, a);
      a += dppf<ROR8>(a);
      a += dppf<ROR4>(a);
      auto sw = __builtin_amdgcn_permlane16_swap(__float_as_uint(a), __float_as_uint(a), false, false);
      const unsigned pr = pack_bf16(__uint_as_float(sw[0]), __uint_as_float(sw[1]));
      __builtin_amdgcn_raw_buffer_store_b128(v4u{pr, dppu<0x55>(pr), dppu<0xAA>(pr), dppu<0xFF>(pr)}, rY, done ? out_off : 1 << 20, 0, AUX_SC1);
      if (done) break;
    }
    return;
  }

  sfor<NSEQ - W_BAR>([&](auto S) { slot(std::integral_constant<int, S.value + W_BAR>{}); });
  sfor<NKV>([&](auto T) { if constexpr (T.value >= PL.pv0[NSEQ]) { pv(T); SCHED(); } });
  if (n < 8) *(v2u*)(lds + ow_off) = v2u{pack_bf16(o[0] * fw, o[1] * fw), pack_bf16(o[2] * fw, o[3] * fw)};
  wg_barrier();
  {
    v4u oa[4];
#pragma unroll
    for (int kb = 0; kb < 4; ++kb) oa[kb] = *(const v4u*)(lds + ork[kb]);
    SCHED();
    f32x4 y0 = {0.f, 0.f, 0.f, 0.f}, y1 = y0;
#pragma unroll
    for (int kb = 0; kb < 4; ++kb) {
      y0 = mfma_pair(oa[kb], wt[kb][0], y0, sel);
      y1 = mfma_pair(oa[kb], wt[kb][1], y1, sel);
    }
    const float u0 = swap16_sum(y0[0] + y0[1], y1[0] + y1[1]), u1 = swap16_sum(y0[2] + y0[3], y1[2] + y1[3]);
    const float v0 = swap32_sum(u0, u0), v1 = swap32_sum(u1, u1);
    __builtin_amdgcn_raw_buffer_store_b128(v4u{__float_as_uint(v0), hi0, __float_as_uint(v1), hi1}, rS, pub_off, 0, AUX_SC1);
  }
}

}

extern "C" void kernel_launch(void* const* d_in, const int* in_sizes, int n_in, void* d_out, int out_size, void* d_ws,
                              size_t ws_size, hipStream_t stream) {
  Params p;
  for (int br = 0; br < 2; ++br) {
    p.br[br].x = d_in[5 * br + 0];
    p.br[br].k = d_in[5 * br + 1];
    p.br[br].v = d_in[5 * br + 2];
    p.br[br].wq = d_in[5 * br + 3];
    p.br[br].wo = d_in[5 * br + 4];
    p.br[br].out = d_out;
    p.br[br].ws = d_ws;
    p.br[br].unused = nullptr;
  }
  hipLaunchKernelGGL(fused_decode_kernel, dim3(NWG), dim3(NTHR), 0, stream, p);
}
```

```cpp
#include <hip/hip_runtime.h>
#include <hip/hip_bf16.h>
#include <stdint.h>
#include <utility>

namespace {

typedef unsigned v4u __attribute__((ext_vector_type(4)));
typedef unsigned v2u __attribute__((ext_vector_type(2)));
typedef unsigned v8u __attribute__((ext_vector_type(8)));
typedef unsigned v16u __attribute__((ext_vector_type(16)));
typedef float f32x4 __attribute__((ext_vector_type(4)));
typedef float f32x2 __attribute__((ext_vector_type(2)));
typedef short s16x8 __attribute__((ext_vector_type(8)));
typedef __bf16 bf16x2 __attribute__((ext_vector_type(2)));
typedef __bf16 bf16x8 __attribute__((ext_vector_type(8)));
typedef __bf16 bf16x16 __attribute__((ext_vector_type(16)));

constexpr int DM = 512, NH = 32, SK = 8192, DH = 128, WIN = 2048;
constexpr int NWG = 256, NWAVE = 8, NTHR = (NWAVE + 1) * 64;
constexpr int NKV = 32;
constexpr int NQ = 4;
constexpr int NW = 16;

constexpr int WS_EPOCH = 0;
constexpr int WS_SEEN = 512;
constexpr int WS_POST = 1024;
constexpr int WS_BOX = 16384;
constexpr int WS_QBOX = WS_BOX + 4 * 64 * 64 * 64;
constexpr int WS_BYTES = WS_QBOX + 64 * 2 * 4 * 1024;

constexpr int W_USE = 8;
constexpr int W_FIRST = 5, W_BAR = 2, W_FIX = 4;
constexpr int Q_LAG = 6, QB_AT = 30, WO_FROM = 20, V_TAIL = 8;
constexpr int TAU_FAST = 1755, TAU_OPEN = 1820, TAU_SLOW = 1970;
constexpr int WL0 = 2, WL1 = 3, WL2 = 4, WL3 = 5;
constexpr int TH1 = -40, TH2 = 0, TH3 = 40;
constexpr int EASE = 20, EASE_N = 10;
constexpr int PACE_FROM = 9, LN_EVERY = 3, ORG0 = 14, MARG0 = 27;
constexpr int NCP = 5, CP_POST[NCP] = {10, 30, 42, 56, 67}, CP_BIAS[NCP] = {0, 0, 0, 9, 0};
constexpr int DL0 = 420, RD0 = 200, DL1 = 115, RD1 = 70, LATE = 30;
constexpr int LAG_LO = -20, LAG_MID = -12, LAG_FAST = -29, AT_SLOW = -25;
constexpr int LAG_A = -16, GAIN_A = 10, LAG_B = -11, LAG_C = -16, LAG_Z = -4, LAG_FP = -36, LAG_D = -28;
constexpr int Q_WAIT = 100, Q_AT = 500;
constexpr int POLL_LAG = 102, POLL_MIN = 40, POLL_MAX = 200;
constexpr int CODE_LEN = 17100;

constexpr int AUX_NT = 2, AUX_SC1 = 16;
constexpr int RSRC_W3 = 0x00020000;
constexpr unsigned SPIN_CAP = 1u << 20;

constexpr int NSEQ = 2 * NKV + NQ + NW, WO_TO = NSEQ - V_TAIL;
constexpr int cmax(int a, int b) { return a > b ? a : b; }
struct Plan {
  int kind[NSEQ], idx[NSEQ], uniq[NSEQ + 1], ops[NSEQ + 1], pos[4][NKV], cnt[4];
  int qp[NSEQ], sc[NSEQ], tr[NSEQ], pv0[NSEQ + 1];
  int at_sm, at_mb;
};
constexpr Plan make_plan() {
  Plan P{};
  for (int s = 0; s < NSEQ; ++s) {
    int k = 1;
    if (s < NQ) k = 0;
    else if (s < NQ + 4) k = 2;
    else if (s >= WO_FROM && s < WO_TO && ((s - WO_FROM + 1) * (NW - 4)) / (WO_TO - WO_FROM) != ((s - WO_FROM) * (NW - 4)) / (WO_TO - WO_FROM)) k = 2;
    else if (P.cnt[1] == NKV) k = 3;
    P.kind[s] = k; P.idx[s] = P.cnt[k]; P.pos[k][P.cnt[k]++] = s;
    P.uniq[s + 1] = P.uniq[s] + (k == 2 && P.idx[s] >= 4 ? 0 : 4);
    P.ops[s + 1] = P.ops[s] + (k == 2 ? 2 : 1);
    P.qp[s] = -1; P.sc[s] = -1; P.tr[s] = -1;
  }
  for (int t = 0; t < NQ; ++t) P.qp[P.pos[0][t] + Q_LAG] = t;
  for (int t = 0; t < NW; ++t) P.tr[P.pos[2][t] + W_USE] = t;
  int at = QB_AT;
  for (int u = 0; u < NKV / 4; ++u) { at = cmax(at + 1, P.pos[1][4 * u + 3] + W_USE); P.sc[at] = u; }
  P.at_sm = at + 1;
  P.at_mb = at + 16;
  int nv = 0;
  for (int s = 0; s < NSEQ; ++s) {
    P.pv0[s] = nv;
    if (s > P.at_sm) { int c = 0; while (nv < NKV && P.pos[3][nv] + (s >= NSEQ - 6 ? WL3 : W_USE) <= s && c < 2) { ++nv; ++c; } }
  }
  P.pv0[NSEQ] = nv;
  return P;
}
constexpr Plan PL = make_plan();
static_assert(PL.cnt[0] == NQ && PL.cnt[1] == NKV && PL.cnt[2] == NW && PL.cnt[3] == NKV && PL.at_mb < NSEQ, "sequence");
constexpr bool rides(int s) { return PL.uniq[s + 1] == PL.uniq[s]; }
constexpr int inflight(int s, int tiles) {
  int j = s, c = 0;
  while (j > 0 && c < tiles) { --j; if (!rides(j)) ++c; }
  return c < tiles ? 63 : PL.ops[s] - PL.ops[j + 1];
}
constexpr int cp_at(int s) { for (int k = 0; k < NCP; ++k) if (CP_POST[k] == s) return k; return -1; }

struct Branch { const void *k, *wq, *v, *wo, *x; void *out, *ws, *unused; };
struct alignas(64) Params { Branch br[2]; };
static_assert(sizeof(Params) == 128, "no padding");

template <class F, int... I> __device__ __forceinline__ void sfor_(F f, std::integer_sequence<int, I...>) { (f(std::integral_constant<int, I>{}), ...); }
template <int N, class F> __device__ __forceinline__ void sfor(F f) { sfor_(f, std::make_integer_sequence<int, N>{}); }

__device__ __forceinline__ f32x4 mfma(v4u a, v4u b, f32x4 c) {
  return __builtin_amdgcn_mfma_f32_16x16x32_bf16(__builtin_bit_cast(s16x8, a), __builtin_bit_cast(s16x8, b), c, 0, 0, 0);
}
__device__ __forceinline__ f32x4 mfma_pair(v4u a, v8u b, f32x4 c, int sel) {
  return __builtin_amdgcn_smfmac_f32_16x16x64_bf16(__builtin_bit_cast(bf16x8, a), __builtin_bit_cast(bf16x16, b), c, sel, 0, 0);
}
template <int CTRL> __device__ __forceinline__ unsigned dppu(unsigned x) {
  return (unsigned)__builtin_amdgcn_mov_dpp((int)x, CTRL, 0xf, 0xf, true);
}
template <int CTRL> __device__ __forceinline__ float dppf(float x) {
  return __builtin_bit_cast(float, __builtin_amdgcn_mov_dpp(__builtin_bit_cast(int, x), CTRL, 0xf, 0xf, true));
}
constexpr int XOR1 = 0xB1, XOR2 = 0x4E, HMIR = 0x141, ROR4 = 0x124, ROR8 = 0x128, BCAST = 0x150;
__device__ __forceinline__ unsigned pack_bf16(float lo, float hi) {
  bf16x2 r = {(__bf16)lo, (__bf16)hi};
  return __builtin_bit_cast(unsigned, r);
}
__device__ __forceinline__ float round_bf16(float x) { return __uint_as_float(pack_bf16(0.f, x) & 0xffff0000u); }
__device__ __forceinline__ float swap16_sum(float a, float b) {
  auto s = __builtin_amdgcn_permlane16_swap(__float_as_uint(a), __float_as_uint(b), false, false);
  return __uint_as_float(s[0]) + __uint_as_float(s[1]);
}
__device__ __forceinline__ float swap32_sum(float a, float b) {
  auto s = __builtin_amdgcn_permlane32_swap(__float_as_uint(a), __float_as_uint(b), false, false);
  return __uint_as_float(s[0]) + __uint_as_float(s[1]);
}
__device__ __forceinline__ float xrow_max(float x) {
  auto s = __builtin_amdgcn_permlane16_swap(__float_as_uint(x), __float_as_uint(x), false, false);
  x = fmaxf(__uint_as_float(s[0]), __uint_as_float(s[1]));
  auto t = __builtin_amdgcn_permlane32_swap(__float_as_uint(x), __float_as_uint(x), false, false);
  return fmaxf(__uint_as_float(t[0]), __uint_as_float(t[1]));
}
__device__ __forceinline__ __amdgpu_buffer_rsrc_t rsrc(unsigned lo, unsigned hi, int bytes) {
  return __builtin_amdgcn_make_buffer_rsrc((void*)(((unsigned long long)hi << 32) | lo), 0, bytes, RSRC_W3);
}
__device__ __forceinline__ unsigned now32() { return (unsigned)__builtin_amdgcn_s_memrealtime(); }
__device__ __forceinline__ void pause_until(unsigned due) {
  for (int d = (int)(now32() - due); d < 0 && d > -800; d = (int)(now32() - due)) __builtin_amdgcn_s_sleep(1);
}
#define SCHED() __builtin_amdgcn_sched_barrier(0)
#define PIN(x) asm volatile("" : "+v"(x))
#define FENCE() asm volatile("" ::: "memory")
__device__ __forceinline__ void wg_barrier() {
  asm volatile("s_waitcnt lgkmcnt(0)\n\ts_barrier" ::: "memory");
}
template <int N> __device__ __forceinline__ void wait_vm() {
  if constexpr (N < 63) asm volatile("s_waitcnt vmcnt(%0)" :: "n"(N) : "memory");
}
template <int C0, int C1, int C2_, int C3, int U, int X> __device__ __forceinline__ void throttle(unsigned org, unsigned tau) {
  asm volatile("s_memrealtime vcc\n\ts_waitcnt lgkmcnt(0)\n\ts_mul_i32 vcc_hi, %1, %6\n\ts_sub_u32 vcc_lo, vcc_lo, %0\n\t"
               "s_lshr_b32 vcc_hi, vcc_hi, 8\n\ts_sub_u32 vcc_lo, vcc_lo, vcc_hi\n\t"
               "s_cmpk_lt_i32 vcc_lo, %9\n\ts_cbranch_scc0 3f\n\t"
               "s_cmpk_lt_i32 vcc_lo, %8\n\ts_cbranch_scc0 2f\n\t"
               "s_cmpk_lt_i32 vcc_lo, %7\n\ts_cbranch_scc0 1f\n\t"
               "s_waitcnt vmcnt(%2)\n1:\n\ts_waitcnt vmcnt(%3)\n2:\n\ts_waitcnt vmcnt(%4)\n3:\n\ts_waitcnt vmcnt(%5)"
               :: "s"(org), "s"(tau), "n"(C0), "n"(C1), "n"(C2_), "n"(C3), "n"(U), "n"(TH1 + X), "n"(TH2 + X), "n"(TH3 + X)
               : "memory", "scc", "vcc");
}
__device__ __forceinline__ void arrive(char* lds, int off, unsigned v) {
  FENCE();
  if ((threadIdx.x & 63) == 0) __hip_atomic_fetch_add((unsigned*)__builtin_assume_aligned(lds + off, 4), v, __ATOMIC_RELAXED, __HIP_MEMORY_SCOPE_WORKGROUP);
  FENCE();
}
__device__ __forceinline__ unsigned await(char* lds, int off, unsigned mask, unsigned want) {
  unsigned v;
  FENCE();
  while (((v = __builtin_amdgcn_readfirstlane(__hip_atomic_load((unsigned*)__builtin_assume_aligned(lds + off, 4), __ATOMIC_RELAXED, __HIP_MEMORY_SCOPE_WORKGROUP))) & mask) != want)
    __builtin_amdgcn_s_sleep(1);
  FENCE();
  return v;
}
__device__ __forceinline__ unsigned mean_post(v2u post, unsigned tag, unsigned late) {
  int d = post.y == tag ? (int)(post.x - late) : 0;
  d += (int)dppu<XOR1>(d); d += (int)dppu<XOR2>(d); d += (int)dppu<HMIR>(d); d += (int)dppu<ROR8>(d);
  auto s = __builtin_amdgcn_permlane16_swap(d, d, false, false);
  d = s[0] + s[1];
  auto t = __builtin_amdgcn_permlane32_swap(d, d, false, false);
  return late + ((int)(t[0] + t[1]) >> 6);
}

constexpr int LDS_Q = 0;
constexpr int LDS_QF = NWAVE * 2 * DH * 4;
constexpr int LDS_ML = LDS_QF + DH * 2;
constexpr int LDS_O = LDS_ML + NWAVE * 8, O_STRIDE = DH * 2 + 16;
constexpr int LDS_LINE = LDS_O + NWAVE * O_STRIDE;
constexpr int LDS_CNT = LDS_LINE + 16;
constexpr int LDS_BYTES = LDS_CNT + 48;

__global__ void __launch_bounds__(NTHR, 3) fused_decode_kernel(Params p) {
  __shared__ __attribute__((aligned(16))) char lds[LDS_BYTES];
  const int bid = blockIdx.x, jj = bid >> 3;
  v16u ka;
  asm volatile("s_load_dwordx16 %0, %1, %2" : "=s"(ka) : "s"(__builtin_amdgcn_kernarg_segment_ptr()), "s"((bid & 4) << 4));
  const int tid = threadIdx.x, lane = tid & 63, w = __builtin_amdgcn_readfirstlane(tid >> 6);
  const int g = lane >> 4, n = lane & 15;
  unsigned cw0 = 0, cw1 = 0, cw2 = 0;
  if (w == NWAVE) {
    unsigned long long pc;
    asm volatile("s_getpc_b64 %0" : "=s"(pc));
    const __amdgpu_buffer_rsrc_t rC = __builtin_amdgcn_make_buffer_rsrc((void*)(pc & ~127ull), 0, CODE_LEN, RSRC_W3);
    cw0 = __builtin_amdgcn_raw_buffer_load_b32(rC, lane * 128, 0, 0);
    cw1 = __builtin_amdgcn_raw_buffer_load_b32(rC, lane * 128 + 8192, 0, 0);
    cw2 = __builtin_amdgcn_raw_buffer_load_b32(rC, lane * 128 + 16384, 0, 0);
  }
  SCHED();
  const int br = (bid >> 2) & 1, head = (bid & 3) * 8 + (jj >> 2), b = (jj >> 1) & 1, half = jj & 1;
  const int G = br * 2 + b, GC = G ^ 2;
  const int me = head * 2 + half;

  const int qrow = 128 * (jj & 3) + 16 * w + 4 * g;
  int qo = w < NWAVE ? qrow * (NH * DH * 2) + head * (DH * 2) + n * 16 : DM * NH * DH * 2;
  asm volatile("s_waitcnt lgkmcnt(0)" : "+s"(ka), "+v"(qo));
  const __amdgpu_buffer_rsrc_t rK = rsrc(ka[0], ka[1], 2 * NH * SK * DH * 2);
  const __amdgpu_buffer_rsrc_t rQ = rsrc(ka[2], ka[3], DM * NH * DH * 2);
  const __amdgpu_buffer_rsrc_t rV = rsrc(ka[4], ka[5], 2 * NH * SK * DH * 2);
  const __amdgpu_buffer_rsrc_t rO = rsrc(ka[6], ka[7], NH * DH * DM * 2);
  const __amdgpu_buffer_rsrc_t rX = rsrc(ka[8], ka[9], 2 * DM * 2);
  const __amdgpu_buffer_rsrc_t rS = rsrc(ka[12], ka[13], WS_BYTES);
  const int kv_off = ((b * NH + head) * SK + (SK - WIN)) * (DH * 2) + half * 32768 + w * 4096;
  const int ko = kv_off + (n >> 2) * 256 + g * 64 + (n & 3) * 16;
  const int vo = kv_off + lane * 16;
  const int oo = (head * DH + 32 * (g >> 1) + (g & 1)) * (DM * 2) + w * 128 + n * 8;
#define KV_SOFF(i) (((i) >> 2) * 65536 + (((i) + head) & 3) * 1024)

  v4u wq[NQ], kk[NKV], vv[NKV], qb[4];
  v8u wt[4][2];
  v2u wo[2 * NW], xd;
  unsigned tag = 0, msk[4], pk[NKV / 4], hi0 = 0, hi1 = 0, org = 0, tau = TAU_OPEN, tb = 0;
  int pub_off = 0, ow_off = 0, ork[4] = {0, 0, 0, 0}, sel = 0;
  float s[NKV / 4], m = 0.f, fw = 0.f;
  f32x4 qa = {0.f, 0.f, 0.f, 0.f}, o = {0.f, 0.f, 0.f, 0.f};
  constexpr float C2 = 1.4426950408889634f;

  auto spread = [&](unsigned x) { return v4u{x & msk[0], x & msk[1], x & msk[2], x & msk[3]}; };
  auto pv = [&](auto T) {
    constexpr int t = T.value;
    o = mfma(vv[t], spread(dppu<BCAST + 4 * (t & 3)>(pk[t >> 2])), o);
    PIN(o);
  };

  auto slot = [&](auto S) {
    constexpr int sq = S.value, kind = PL.kind[sq], i = PL.idx[sq];
    if constexpr (sq < W_FIRST || rides(sq)) {
    } else if constexpr (sq <= PACE_FROM) wait_vm<inflight(sq, W_FIX) + (sq < W_FIRST + W_FIX ? 1 : 0)>();
    else throttle<inflight(sq, WL0), inflight(sq, WL1), inflight(sq, WL2), inflight(sq, WL3), PL.uniq[sq] - 4,
                  cmax(sq + 1 - (NSEQ - EASE_N), 0) * EASE / EASE_N>(org, tau);
    if constexpr (kind == 0) wq[i] = __builtin_amdgcn_raw_buffer_load_b128(rQ, qo, i * (NH * DH * 2), AUX_NT);
    if constexpr (kind == 1) kk[i] = __builtin_amdgcn_raw_buffer_load_b128(rK, ko, KV_SOFF(i), AUX_NT);
    if constexpr (kind == 2) {
      constexpr int kb = i >> 2, u = i & 3, r0 = 64 * (u >> 1) + 16 * (u & 1);
      const int kr = (kb + jj) & 3;
      wo[2 * i] = __builtin_amdgcn_raw_buffer_load_b64(rO, oo, (r0 + 2 * kr) * 1024, 0);
      wo[2 * i + 1] = __builtin_amdgcn_raw_buffer_load_b64(rO, oo, (r0 + 8 + 2 * kr) * 1024, 0);
    }
    if constexpr (kind == 3) vv[i] = __builtin_amdgcn_raw_buffer_load_b128(rV, vo, KV_SOFF(i), AUX_NT);
    SCHED();
    if constexpr (sq == W_FIRST - 1) {
      xd = __builtin_amdgcn_raw_buffer_load_b64(rX, ((n >> 3) * DM + qrow) * 2, 0, 0);
#pragma unroll
      for (int j = 0; j < 4; ++j) msk[j] = ((n & 7) >> 1) == j ? ((n & 1) ? 0xffff0000u : 0x0000ffffu) : 0u;
      tb = __builtin_amdgcn_readfirstlane(*(const unsigned*)(lds + LDS_LINE + 12));
    }
    if constexpr (cp_at(sq) >= 0) arrive(lds, LDS_CNT + 16 + 4 * cp_at(sq), ((now32() - tb) << 4) + 1u);
    if constexpr (sq >= PACE_FROM && (sq - PACE_FROM) % LN_EVERY == 0) {
      FENCE();
      const v2u ln = *(const v2u*)(lds + LDS_LINE);
      org = __builtin_amdgcn_readfirstlane(ln.x);
      tau = __builtin_amdgcn_readfirstlane(ln.y);
    }
    if constexpr (PL.qp[sq] >= 0) {
      constexpr int t = PL.qp[sq];
      qa = mfma(wq[t], spread(__builtin_amdgcn_perm(xd[t >> 1], xd[t >> 1], (t & 1) ? 0x03020302u : 0x01000100u)), qa);
      PIN(qa);
      if constexpr (t == NQ - 1) {
#pragma unroll
        for (int j = 0; j < 4; ++j) ((float*)(lds + LDS_Q))[(2 * w + (n >> 3)) * DH + 32 * g + 8 * j + (n & 7)] = qa[j];
        arrive(lds, LDS_CNT, 1u);
      }
    }
    if constexpr (sq == QB_AT) {
      await(lds, LDS_CNT + 4, ~0u, 1u);
      const v4u q = *(const v4u*)(lds + LDS_QF + (32 * g + 8 * (n & 3)) * 2);
      tag = __builtin_amdgcn_readfirstlane(*(const unsigned*)(lds + LDS_LINE + 8));
      const v4u z = {0u, 0u, 0u, 0u};
#pragma unroll
      for (int t = 0; t < 4; ++t) qb[t] = ((n >> 2) == t) ? q : z;
    }
    if constexpr (PL.sc[sq] >= 0) {
      constexpr int u = PL.sc[sq];
      f32x4 d = {0.f, 0.f, 0.f, 0.f};
#pragma unroll
      for (int t = 0; t < 4; ++t) d = mfma(kk[4 * u + t], qb[t], d);
      const float raw = (dppf<0x00>(d[0]) + dppf<0x55>(d[1])) + (dppf<0xAA>(d[2]) + dppf<0xFF>(d[3]));
      s[u] = round_bf16(round_bf16(raw) * 0.088388347648318440f);
      PIN(s[u]);
    }
    if constexpr (sq == PL.at_sm) {
      m = s[0];
#pragma unroll
      for (int u = 1; u < NKV / 4; ++u) m = fmaxf(m, s[u]);
      m = fmaxf(m, dppf<ROR4>(m));
      m = fmaxf(m, dppf<ROR8>(m));
      m = xrow_max(m) * C2;
      float l = 0.f;
#pragma unroll
      for (int u = 0; u < NKV / 4; ++u) {
        const float e = __builtin_amdgcn_exp2f(fmaf(s[u], C2, -m));
        l += e;
        pk[u] = pack_bf16(e, e);
        PIN(pk[u]);
      }
      l += dppf<ROR4>(l);
      l += dppf<ROR8>(l);
      l = swap16_sum(l, l);
      l = swap32_sum(l, l);
      if (lane == 0) *(f32x2*)(lds + LDS_ML + w * 8) = f32x2{m, l};
      arrive(lds, LDS_CNT + 8, 1u);
    }
    if constexpr (sq == PL.at_mb) {
      await(lds, LDS_CNT + 8, ~0u, NWAVE);
      const f32x2 r = *(const f32x2*)(lds + LDS_ML + (lane & 7) * 8);
      float M = fmaxf(r[0], dppf<XOR1>(r[0]));
      M = fmaxf(M, dppf<XOR2>(M));
      M = fmaxf(M, dppf<HMIR>(M));
      float L = r[1] * __builtin_amdgcn_exp2f(r[0] - M);
      PIN(L);
      L += dppf<XOR1>(L);
      L += dppf<XOR2>(L);
      L += dppf<HMIR>(L);
      fw = __builtin_amdgcn_exp2f(m - M) / L;
      const unsigned lse = __float_as_uint(M + __builtin_amdgcn_logf(L));
      hi0 = (tag << 16) | (lse & 0xffffu);
      hi1 = (tag & 0xffff0000u) | (lse >> 16);
      pub_off = lane < 32 ? WS_BOX + ((G * 64 + 8 * w + (n >> 1)) * 64 + me) * 64 + (2 * (n & 1) + g) * 16 : WS_BYTES;
      ow_off = LDS_O + w * O_STRIDE + g * 64 + n * 8;
#pragma unroll
      for (int kb = 0; kb < 4; ++kb) { ork[kb] = LDS_O + (2 * (n >> 2) + (n & 1)) * O_STRIDE + g * 64 + ((kb + jj) & 3) * 16; PIN(ork[kb]); }
      sel = (n & 2) ? 0xEEEE : 0x4444;
      PIN(fw); PIN(hi0); PIN(hi1); PIN(pub_off); PIN(ow_off); PIN(sel);
    }
    if constexpr (PL.pv0[sq + 1] > PL.pv0[sq]) pv(std::integral_constant<int, PL.pv0[sq]>{});
    if constexpr (PL.pv0[sq + 1] > PL.pv0[sq] + 1) pv(std::integral_constant<int, PL.pv0[sq] + 1>{});
    if constexpr (PL.tr[sq] >= 0) {
      constexpr int t = PL.tr[sq];
#pragma unroll
      for (int c = 0; c < 4; ++c) {
        unsigned r = __builtin_amdgcn_perm(wo[2 * t + 1][c >> 1], wo[2 * t][c >> 1], (c & 1) ? 0x07060302u : 0x05040100u);
        PIN(r);
        wt[t >> 2][c >> 1][2 * (t & 3) + (c & 1)] = r;
      }
    }
    SCHED();
  };

  sfor<W_BAR>(slot);
  if (w == NWAVE) {
    tb = now32();
    if (lane < 12) *(unsigned*)(lds + LDS_CNT + lane * 4) = 0u;
    if (lane == 0) *(v4u*)(lds + LDS_LINE) = v4u{tb + ORG0, TAU_OPEN, 0u, tb};
  }
  wg_barrier();

  if (__builtin_expect(w == NWAVE, 0)) {
    const __amdgpu_buffer_rsrc_t rY = rsrc(ka[10], ka[11], 2 * 2 * DM * 2);
    const int out_at = (b * (2 * DM) + (br ^ 1) * DM + me * 8) * 2, grp = br * NH + head;
    const unsigned epoch = __builtin_amdgcn_raw_buffer_load_b32(rS, WS_EPOCH, 0, AUX_SC1);
    unsigned touch = __builtin_amdgcn_raw_buffer_load_b32(rY, out_at, 0, 0);
    PIN(touch); PIN(cw0); PIN(cw1); PIN(cw2);
    tag = __builtin_amdgcn_readfirstlane(epoch) + 0x00010001u;
    org = tb + ORG0;
    unsigned dl = 0;
    int at = -MARG0;
    bool sped = false, slowed = false;
    auto post = [&](auto K) {
      constexpr int k = K.value;
      dl = k == 0 ? tb + DL0 : org + ((tau * PL.uniq[CP_POST[k]]) >> 8) + DL1;
      const unsigned tcp = tb + (await(lds, LDS_CNT + 16 + 4 * k, 15u, NWAVE) >> 7);
      if ((jj & 3) == (k & 3) && (int)(now32() - dl) < 0)
        __builtin_amdgcn_raw_buffer_store_b64(v2u{tcp, tag}, rS, lane == 0 ? WS_POST + k * 1024 + grp * 16 : WS_BYTES, 0, AUX_SC1);
    };
    auto line = [&](auto K) {
      constexpr int k = K.value, U = PL.uniq[CP_POST[k]];
      pause_until(dl + (k == 0 ? RD0 : RD1));
      FENCE();
      const unsigned tm = CP_BIAS[k] + __builtin_amdgcn_readfirstlane(
          mean_post(__builtin_amdgcn_raw_buffer_load_b64(rS, WS_POST + k * 1024 + lane * 16, 0, AUX_SC1), tag, dl + LATE));
      if constexpr (k > 0) {
        const int lag = (int)(tm - org - ((tau * U) >> 8));
        constexpr int per = 65536 / (U - PL.uniq[CP_POST[k - 1]]);
        int t2 = tau;
        if constexpr (k == 1) t2 = TAU_FAST + max(lag - LAG_A, 0) * GAIN_A;
        else if (lag > (k == NCP - 1 ? LAG_Z : LAG_B)) t2 += sped ? 0 : ((lag - LAG_C) * per * 7) >> 10;
        else if (slowed) t2 += lag < LAG_FP ? ((lag - LAG_D) * per * 3) >> 10 : 0;
        else if (lag < LAG_FAST) t2 -= 60;
        t2 = min(max(t2, TAU_FAST), TAU_SLOW);
        slowed = t2 > (int)tau;
        at = slowed ? AT_SLOW : t2 < (int)tau ? LAG_LO - (((int)tau - t2) >> 3) : min(max(lag, LAG_LO), LAG_MID);
        sped = k > 1 && t2 < (int)tau;
        tau = t2;
      }
      org = tm - at - ((tau * U) >> 8);
      if (lane == 0) *(v2u*)(lds + LDS_LINE) = v2u{org, tau};
      FENCE();
    };
    await(lds, LDS_CNT, ~0u, NWAVE);
    const int qbox = WS_QBOX + grp * 8192 + lane * 16;
#pragma unroll
    for (int bb = 0; bb < 2; ++bb) {
      f32x2 a[NWAVE];
#pragma unroll
      for (int ww = 0; ww < NWAVE; ++ww) a[ww] = *(const f32x2*)(lds + LDS_Q + ((2 * ww + bb) * DH + 2 * lane) * 4);
      const f32x2 t = ((a[0] + a[1]) + (a[2] + a[3])) + ((a[4] + a[5]) + (a[6] + a[7]));
      __builtin_amdgcn_raw_buffer_store_b128(v4u{__float_as_uint(t[0]), tag, __float_as_uint(t[1]), tag}, rS, qbox + bb * 4096 + (jj & 3) * 1024, 0, AUX_SC1);
    }
    const unsigned tq = now32();
    post(std::integral_constant<int, 0>{});
    pause_until(tq + Q_WAIT);
    pause_until(tb + Q_AT);
    v4u r[4];
    for (unsigned spins = 0;; ++spins) {
      FENCE();
#pragma unroll
      for (int j = 0; j < 4; ++j) r[j] = __builtin_amdgcn_raw_buffer_load_b128(rS, qbox + b * 4096, j * 1024, AUX_SC1);
      unsigned bad = 0;
#pragma unroll
      for (int j = 0; j < 4; ++j) bad |= (r[j].y ^ tag) | (r[j].w ^ tag);
      if (__all(bad == 0) || spins > SPIN_CAP) break;
      __builtin_amdgcn_s_sleep(8);
    }
    *(unsigned*)(lds + LDS_QF + lane * 4) = pack_bf16((__uint_as_float(r[0].x) + __uint_as_float(r[1].x)) + (__uint_as_float(r[2].x) + __uint_as_float(r[3].x)),
                                                      (__uint_as_float(r[0].z) + __uint_as_float(r[1].z)) + (__uint_as_float(r[2].z) + __uint_as_float(r[3].z)));
    if (lane == 0) *(unsigned*)(lds + LDS_LINE + 8) = tag;
    arrive(lds, LDS_CNT + 4, 1u);
    if ((jj & 3) == 1) __builtin_amdgcn_raw_buffer_store_b64(v2u{tag, ~tag}, rS, lane == 0 ? WS_SEEN + grp * 8 : WS_BYTES, 0, AUX_SC1);
    line(std::integral_constant<int, 0>{});
    sfor<NCP - 1>([&](auto K) {
      post(std::integral_constant<int, K.value + 1>{});
      if (K.value == NCP - 3 && bid == 0) {
#pragma unroll 1
        for (unsigned spins = 0; spins < SPIN_CAP; ++spins) {
          FENCE();
          const v2u t = __builtin_amdgcn_raw_buffer_load_b64(rS, WS_SEEN + lane * 8, 0, AUX_SC1);
          if (__all(t.x == tag && t.y == ~tag)) break;
          __builtin_amdgcn_s_sleep(8);
        }
      }
      line(std::integral_constant<int, K.value + 1>{});
    });
    int box = WS_BOX + (GC * 64 + me) * 4096 + (lane >> 2) * 128 + (lane & 3) * 16, out_off = lane == 0 ? out_at : 1 << 20;
    PIN(box); PIN(out_off);
    unsigned due = __builtin_amdgcn_readfirstlane(org + ((tau * PL.uniq[NSEQ]) >> 8) + POLL_LAG);
    asm volatile("" : "+s"(due));
    wg_barrier();
    if (bid == 0) __builtin_amdgcn_raw_buffer_store_b32(tag, rS, lane == 0 ? WS_EPOCH : WS_BYTES, 0, AUX_SC1);
    {
      const unsigned t = now32(), first = t + __builtin_amdgcn_readfirstlane(min(max((int)(due - t), POLL_MIN), POLL_MAX));
      while ((int)(now32() - first) < 0) {}
    }
    for (unsigned spins = 0;; ++spins) {
      FENCE();
#pragma unroll
      for (int j = 0; j < 4; ++j) r[j] = __builtin_amdgcn_raw_buffer_load_b128(rS, box, (j >> 1) * 2048 + (j & 1) * 64, AUX_SC1);
      unsigned bad = 0;
#pragma unroll
      for (int j = 0; j < 4; ++j) bad |= __builtin_amdgcn_perm(r[j].w, r[j].y, 0x07060302u) ^ tag;
      const bool done = __builtin_amdgcn_ballot_w64(bad != 0) == 0 || spins > SPIN_CAP;
      float a0 = 0.f, a1 = 0.f;
#pragma unroll
      for (int h = 0; h < 2; ++h) {
        const float l0 = __uint_as_float(__builtin_amdgcn_perm(r[2 * h].w, r[2 * h].y, 0x05040100u));
        const float l1 = __uint_as_float(__builtin_amdgcn_perm(r[2 * h + 1].w, r[2 * h + 1].y, 0x05040100u));
        const float w0 = __builtin_amdgcn_rcpf(1.f + __builtin_amdgcn_exp2f(l1 - l0));
        const float x1 = __uint_as_float(r[2 * h + 1].x), z1 = __uint_as_float(r[2 * h + 1].z);
        const float c0 = fmaf(w0, __uint_as_float(r[2 * h].x) - x1, x1), c1 = fmaf(w0, __uint_as_float(r[2 * h].z) - z1, z1);
        a0 = h ? a0 + c0 : c0;
        a1 = h ? a1 + c1 : c1;
      }
      float a = swap16_sum(a0, a1);
      a = swap32_sum(a, a);
      a += dppf<ROR8>(a);
      a += dppf<ROR4>(a);
      auto sw = __builtin_amdgcn_permlane16_swap(__float_as_uint(a), __float_as_uint(a), false, false);
      const unsigned pr = pack_bf16(__uint_as_float(sw[0]), __uint_as_float(sw[1]));
      __builtin_amdgcn_raw_buffer_store_b128(v4u{pr, dppu<0x55>(pr), dppu<0xAA>(pr), dppu<0xFF>(pr)}, rY, done ? out_off : 1 << 20, 0, AUX_SC1);
      if (done) break;
    }
    return;
  }

  sfor<NSEQ - W_BAR>([&](auto S) { slot(std::integral_constant<int, S.value + W_BAR>{}); });
  sfor<NKV>([&](auto T) { if constexpr (T.value >= PL.pv0[NSEQ]) { pv(T); SCHED(); } });
  if (n < 8) *(v2u*)(lds + ow_off) = v2u{pack_bf16(o[0] * fw, o[1] * fw), pack_bf16(o[2] * fw, o[3] * fw)};
  wg_barrier();
  {
    v4u oa[4];
#pragma unroll
    for (int kb = 0; kb < 4; ++kb) oa[kb] = *(const v4u*)(lds + ork[kb]);
    SCHED();
    f32x4 y0 = {0.f, 0.f, 0.f, 0.f}, y1 = y0;
#pragma unroll
    for (int kb = 0; kb < 4; ++kb) {
      y0 = mfma_pair(oa[kb], wt[kb][0], y0, sel);
      y1 = mfma_pair(oa[kb], wt[kb][1], y1, sel);
    }
    const float u0 = swap16_sum(y0[0] + y0[1], y1[0] + y1[1]), u1 = swap16_sum(y0[2] + y0[3], y1[2] + y1[3]);
    const float v0 = swap32_sum(u0, u0), v1 = swap32_sum(u1, u1);
    __builtin_amdgcn_raw_buffer_store_b128(v4u{__float_as_uint(v0), hi0, __float_as_uint(v1), hi1}, rS, pub_off, 0, AUX_SC1);
  }
}

}

extern "C" void kernel_launch(void* const* d_in, const int* in_sizes, int n_in, void* d_out, int out_size, void* d_ws,
                              size_t ws_size, hipStream_t stream) {
  Params p;
  for (int br = 0; br < 2; ++br) {
    p.br[br].x = d_in[5 * br + 0];
    p.br[br].k = d_in[5 * br + 1];
    p.br[br].v = d_in[5 * br + 2];
    p.br[br].wq = d_in[5 * br + 3];
    p.br[br].wo = d_in[5 * br + 4];
    p.br[br].out = d_out;
    p.br[br].ws = d_ws;
    p.br[br].unused = nullptr;
  }
  hipLaunchKernelGGL(fused_decode_kernel, dim3(NWG), dim3(NTHR), 0, stream, p);
}
```
